```python
import torch

def _softmax(x, axis=-1):
    return torch.softmax(x, dim=axis)
def _gelu(x):
    return torch.nn.functional.gelu(x, approximate="tanh")
def _silu(x):
    return torch.nn.functional.silu(x)
def _sigmoid(x):
    return torch.sigmoid(x)
def _layernorm(x, axis=-1, eps=1e-5):
    m = x.mean(dim=axis, keepdim=True)
    v = x.var(dim=axis, keepdim=True, unbiased=False)
    return (x - m) / torch.sqrt(v + eps)
def _scatter_add(tgt, idx, src, axis=0):
    return tgt.clone().index_add_(axis, idx, src)
def _segment_sum(src, seg, n):
    out = torch.zeros((n,) + src.shape[1:], dtype=src.dtype)
    return out.index_add_(0, seg, src)
def _pad(x, pw):
    flat = tuple(v for pair in reversed(pw) for v in pair)
    return torch.nn.functional.pad(x, flat)

kernel_name = 'interleave_layers__gqa_block_cached__glu_gated_swiglu__decode'

def setup_inputs():

    _w = (lambda a: (lambda t: t.to(torch.bfloat16) if t.is_floating_point() else t)(torch.as_tensor(a)))
    def _A_setup():
        B, Sq, D = 4, 1, 576
        Hq, Hkv, Sk, Dh = 32, 16, 4096, 128
        assert Hq % Hkv == 0, (Hq, Hkv)
        return {
            "x": _w((lambda *s: torch.randn(*s, dtype=torch.bfloat16) * 0.05)(B, Sq, D)),
            "k_cache": _w((lambda *s: torch.randn(*s, dtype=torch.bfloat16) * 0.8)(B, Hkv, Sk, Dh)),
            "v_cache": _w((lambda *s: torch.randn(*s, dtype=torch.bfloat16) * 0.8)(B, Hkv, Sk, Dh)),
            "wq": _w((lambda *s: torch.randn(*s, dtype=torch.bfloat16) * 0.8)(D, Hq * Dh) * (D ** -0.5)),
            "wo": _w((lambda *s: torch.randn(*s, dtype=torch.bfloat16) * 0.8)(Hq * Dh, D) * ((Hq * Dh) ** -0.5)),
        }
    _A_in = _A_setup()

    def _B_setup():
        B, S, D, F = 4, 1, 576, 4608
        return {
            "x": _w((lambda *s: torch.randn(*s, dtype=torch.bfloat16) * 0.05)(B, S, D)),
            "w1": _w((lambda *s: torch.randn(*s, dtype=torch.bfloat16) * 0.2)(D, F) * (2.0 / D) ** 0.5),
            "w3": _w((lambda *s: torch.randn(*s, dtype=torch.bfloat16) * 0.2)(D, F) * (2.0 / D) ** 0.5),
            "w2": _w((lambda *s: torch.randn(*s, dtype=torch.bfloat16) * 0.2)(F, D) * (2.0 / F) ** 0.5),
        }
    _B_in = _B_setup()

    return {
        "A_x": _A_in["x"],
        "A_k_cache": _A_in["k_cache"],
        "A_v_cache": _A_in["v_cache"],
        "A_wq": _A_in["wq"],
        "A_wo": _A_in["wo"],
        "B_x": _B_in["x"],
        "B_w1": _B_in["w1"],
        "B_w3": _B_in["w3"],
        "B_w2": _B_in["w2"],
    }

def reference(A_x, A_k_cache, A_v_cache, A_wq, A_wo, B_x, B_w1, B_w3, B_w2):
    def _A_ref(x, k_cache, v_cache, wq, wo):
        B, Sq, D = x.shape
        Hq, Dh = 32, 128
        Hkv = k_cache.shape[1]
        rep = Hq // Hkv
        q = (lambda x, *d: x.permute(*d))((x @ wq).reshape(B, Sq, Hq, Dh), 0, 2, 1, 3)
        k = (lambda x, n, axis: torch.repeat_interleave(x, n, dim=axis))(k_cache, rep, axis=1)
        v = (lambda x, n, axis: torch.repeat_interleave(x, n, dim=axis))(v_cache, rep, axis=1)
        scores = torch.einsum("bhqd,bhkd->bhqk", q, k) * (Dh ** -0.5)
        p = _softmax(scores, axis=-1)
        o = torch.einsum("bhqk,bhkd->bhqd", p, v)
        return (lambda x, *d: x.permute(*d))(o, 0, 2, 1, 3).reshape(B, Sq, Hq * Dh) @ wo

    def _B_ref(x, w1, w3, w2):
        gate = torch.einsum("bsd,df->bsf", x, w1)
        up = torch.einsum("bsd,df->bsf", x, w3)
        h = _silu(gate) * up
        out = torch.einsum("bsf,fd->bsd", h, w2)
        return out

    x = A_x
    x = x + _A_ref(_layernorm(x, axis=-1), A_k_cache, A_v_cache, A_wq, A_wo)
    x = x + _B_ref(_layernorm(x, axis=-1), B_w1, B_w3, B_w2)
    x = x + _A_ref(_layernorm(x, axis=-1), A_k_cache, A_v_cache, A_wq, A_wo)
    x = x + _B_ref(_layernorm(x, axis=-1), B_w1, B_w3, B_w2)
    return x
```

```cpp
#include <hip/hip_runtime.h>
#include <hip/hip_bf16.h>
#include <stdint.h>

typedef unsigned short u16;
typedef unsigned v4u __attribute__((ext_vector_type(4)));
constexpr int B = 4, D = 576, HQ = 32, HKV = 16, SK = 4096, DH = 128, F = 4608, QD = HQ * DH;
constexpr int NSPLIT = 4;

__device__ __forceinline__ float bf2f(unsigned b) { return __builtin_bit_cast(float, b << 16); }
__device__ __forceinline__ float bflo(unsigned w) { return __builtin_bit_cast(float, w << 16); }
__device__ __forceinline__ float bfhi(unsigned w) { return __builtin_bit_cast(float, w & 0xffff0000u); }
__device__ __forceinline__ unsigned f2bf(float f) {
  unsigned u = __builtin_bit_cast(unsigned, f);
  if ((u & 0x7fffffffu) > 0x7f800000u) return 0x7fc0u;
  u += 0x7fffu + ((u >> 16) & 1u);
  return u >> 16;
}
__device__ __forceinline__ float rbf(float f) { return bf2f(f2bf(f)); }
__device__ __forceinline__ float wsum(float v) {
  for (int o = 32; o > 0; o >>= 1) v += __shfl_xor(v, o);
  return v;
}
__device__ __forceinline__ float wmax(float v) {
  for (int o = 32; o > 0; o >>= 1) v = fmaxf(v, __shfl_xor(v, o));
  return v;
}
__device__ __forceinline__ void ln_rows(const float* x, float* ln) {
  const int w = threadIdx.x >> 6, l = threadIdx.x & 63;
  float v[9], s = 0.f;
  for (int i = 0; i < 9; ++i) { v[i] = x[w * D + l + 64 * i]; s += v[i]; }
  const float m = wsum(s) * (1.f / D);
  float q = 0.f;
  for (int i = 0; i < 9; ++i) { v[i] -= m; q += v[i] * v[i]; }
  const float r = 1.f / sqrtf(wsum(q) * (1.f / D) + 1e-5f);
  for (int i = 0; i < 9; ++i) ln[w * D + l + 64 * i] = rbf(v[i] * r);
}

__global__ void k_init(const u16* x, float* xf) {
  int i = blockIdx.x * 256 + threadIdx.x;
  if (i < B * D) xf[i] = bf2f(x[i]);
}
__global__ __launch_bounds__(256) void k_q(const float* x, const u16* wq, float* q) {
  __shared__ float ln[B * D];
  __shared__ float red[4][B][64];
  ln_rows(x, ln);
  __syncthreads();
  const int w = threadIdx.x >> 6, l = threadIdx.x & 63, c = blockIdx.x * 64 + l;
  float a[B] = {0, 0, 0, 0};
  for (int k = w * 144; k < w * 144 + 144; ++k) {
    const float wv = bf2f(wq[(size_t)k * QD + c]);
    for (int b = 0; b < B; ++b) a[b] += ln[b * D + k] * wv;
  }
  for (int b = 0; b < B; ++b) red[w][b][l] = a[b];
  __syncthreads();
  q[w * QD + c] = rbf(red[0][w][l] + red[1][w][l] + red[2][w][l] + red[3][w][l]);
}
__global__ __launch_bounds__(256) void k_attn(const float* q, const u16* kc, const u16* vc, float* po, float* pml) {
  __shared__ float sc[2][1024];
  __shared__ float red[16][2][128];
  __shared__ float r2[2][4];
  const int wg = blockIdx.x, s = wg & 3, bh = wg >> 2, b = bh >> 4, hk = bh & 15;
  const int t = threadIdx.x, l = t & 63, w = t >> 6, r = t & 15, g = t >> 4;
  const v4u* K = (const v4u*)(kc + ((size_t)bh * SK + s * 1024) * DH);
  const v4u* V = (const v4u*)(vc + ((size_t)bh * SK + s * 1024) * DH);
  float q0[8], q1[8];
  for (int j = 0; j < 8; ++j) {
    q0[j] = q[b * QD + (2 * hk) * DH + r * 8 + j];
    q1[j] = q[b * QD + (2 * hk + 1) * DH + r * 8 + j];
  }
#pragma unroll 8
  for (int i = 0; i < 64; ++i) {
    const int key = i * 16 + g;
    const v4u kv = __builtin_nontemporal_load(K + key * 16 + r);
    float a0 = 0.f, a1 = 0.f;
    for (int j = 0; j < 4; ++j) {
      a0 += bflo(kv[j]) * q0[2 * j] + bfhi(kv[j]) * q0[2 * j + 1];
      a1 += bflo(kv[j]) * q1[2 * j] + bfhi(kv[j]) * q1[2 * j + 1];
    }
    for (int o = 8; o > 0; o >>= 1) { a0 += __shfl_xor(a0, o); a1 += __shfl_xor(a1, o); }
    if (r == 0) { sc[0][key] = a0 * 0.08838834764831845f; sc[1][key] = a1 * 0.08838834764831845f; }
  }
  __syncthreads();
  float m[2], ls[2];
  for (int h = 0; h < 2; ++h) {
    float mm = -INFINITY;
    for (int i = 0; i < 4; ++i) mm = fmaxf(mm, sc[h][t + 256 * i]);
    mm = wmax(mm);
    if (l == 0) r2[h][w] = mm;
  }
  __syncthreads();
  for (int h = 0; h < 2; ++h) m[h] = fmaxf(fmaxf(r2[h][0], r2[h][1]), fmaxf(r2[h][2], r2[h][3]));
  __syncthreads();
  for (int h = 0; h < 2; ++h) {
    float ss = 0.f;
    for (int i = 0; i < 4; ++i) { const float p = __expf(sc[h][t + 256 * i] - m[h]); sc[h][t + 256 * i] = p; ss += p; }
    ss = wsum(ss);
    if (l == 0) r2[h][w] = ss;
  }
  __syncthreads();
  for (int h = 0; h < 2; ++h) ls[h] = r2[h][0] + r2[h][1] + r2[h][2] + r2[h][3];
  float o0[8] = {0}, o1[8] = {0};
#pragma unroll 8
  for (int i = 0; i < 64; ++i) {
    const int key = i * 16 + g;
    const v4u vv = __builtin_nontemporal_load(V + key * 16 + r);
    const float p0 = sc[0][key], p1 = sc[1][key];
    for (int j = 0; j < 4; ++j) {
      o0[2 * j] += p0 * bflo(vv[j]); o0[2 * j + 1] += p0 * bfhi(vv[j]);
      o1[2 * j] += p1 * bflo(vv[j]); o1[2 * j + 1] += p1 * bfhi(vv[j]);
    }
  }
  for (int j = 0; j < 8; ++j) { red[g][0][r * 8 + j] = o0[j]; red[g][1][r * 8 + j] = o1[j]; }
  __syncthreads();
  {
    const int h = t >> 7, d = t & 127;
    float a = 0.f;
    for (int gg = 0; gg < 16; ++gg) a += red[gg][h][d];
    po[(wg * 2 + h) * 128 + d] = a;
    if (d == 0) { pml[(wg * 2 + h) * 2] = m[h]; pml[(wg * 2 + h) * 2 + 1] = ls[h]; }
  }
}
__global__ __launch_bounds__(256) void k_comb(const float* po, const float* pml, float* o) {
  const int bh = blockIdx.x, b = bh >> 4, hk = bh & 15, t = threadIdx.x, h = t >> 7, d = t & 127;
  float M = -INFINITY;
  for (int s = 0; s < 4; ++s) M = fmaxf(M, pml[((bh * 4 + s) * 2 + h) * 2]);
  float L = 0.f, a = 0.f;
  for (int s = 0; s < 4; ++s) {
    const float e = __expf(pml[((bh * 4 + s) * 2 + h) * 2] - M);
    L += e * pml[((bh * 4 + s) * 2 + h) * 2 + 1];
    a += e * po[((bh * 4 + s) * 2 + h) * 128 + d];
  }
  o[b * QD + (2 * hk + h) * DH + d] = rbf(a / L);
}
template <int KD, int KS>
__global__ __launch_bounds__(256) void k_down(const float* in, const u16* wgt, float* part) {
  __shared__ float red[4][B][64];
  constexpr int KC = KD / KS / 4;
  const int w = threadIdx.x >> 6, l = threadIdx.x & 63, c = blockIdx.x * 64 + l, k0 = (blockIdx.y * 4 + w) * KC;
  float a[B] = {0, 0, 0, 0};
  for (int k = k0; k < k0 + KC; ++k) {
    const float wv = bf2f(wgt[(size_t)k * D + c]);
    for (int b = 0; b < B; ++b) a[b] += in[b * KD + k] * wv;
  }
  for (int b = 0; b < B; ++b) red[w][b][l] = a[b];
  __syncthreads();
  part[(blockIdx.y * B + w) * D + c] = red[0][w][l] + red[1][w][l] + red[2][w][l] + red[3][w][l];
}
template <int KS>
__global__ void k_res(const float* xin, const float* part, float* xout, u16* out) {
  int i = blockIdx.x * 256 + threadIdx.x;
  if (i >= B * D) return;
  float a = 0.f;
  for (int s = 0; s < KS; ++s) a += part[s * B * D + i];
  const float r = rbf(xin[i] + rbf(a));
  xout[i] = r;
  if (out) out[i] = (u16)f2bf(r);
}
__global__ __launch_bounds__(256) void k_gu(const float* x, const u16* w1, const u16* w3, float* h) {
  __shared__ float ln[B * D];
  __shared__ float red[2][4][B][64];
  ln_rows(x, ln);
  __syncthreads();
  const int w = threadIdx.x >> 6, l = threadIdx.x & 63, c = blockIdx.x * 64 + l;
  float a[B] = {0, 0, 0, 0}, u[B] = {0, 0, 0, 0};
  for (int k = w * 144; k < w * 144 + 144; ++k) {
    const float wv = bf2f(w1[(size_t)k * F + c]), uv = bf2f(w3[(size_t)k * F + c]);
    for (int b = 0; b < B; ++b) { a[b] += ln[b * D + k] * wv; u[b] += ln[b * D + k] * uv; }
  }
  for (int b = 0; b < B; ++b) { red[0][w][b][l] = a[b]; red[1][w][b][l] = u[b]; }
  __syncthreads();
  const float g = rbf(red[0][0][w][l] + red[0][1][w][l] + red[0][2][w][l] + red[0][3][w][l]);
  const float up = rbf(red[1][0][w][l] + red[1][1][w][l] + red[1][2][w][l] + red[1][3][w][l]);
  h[w * F + c] = rbf(rbf(g / (1.f + __expf(-g))) * up);
}

extern "C" void kernel_launch(void* const* d_in, const int* in_sizes, int n_in, void* d_out, int out_size, void* d_ws,
                              size_t ws_size, hipStream_t stream) {
  const u16* x = (const u16*)d_in[0];
  const u16* kc = (const u16*)d_in[1];
  const u16* vc = (const u16*)d_in[2];
  const u16* wq = (const u16*)d_in[3];
  const u16* wo = (const u16*)d_in[4];
  const u16* w1 = (const u16*)d_in[6];
  const u16* w3 = (const u16*)d_in[7];
  const u16* w2 = (const u16*)d_in[8];
  float* ws = (float*)d_ws;
  float* xa = ws;
  float* xb = ws + 4096;
  float* q = ws + 8192;
  float* o = q + 16384;
  float* h = o + 16384;
  float* po = h + 18432;
  float* pml = po + 65536;
  float* part = pml + 1024;
  k_init<<<9, 256, 0, stream>>>(x, xa);
  for (int layer = 0; layer < 2; ++layer) {
    k_q<<<64, 256, 0, stream>>>(xa, wq, q);
    k_attn<<<256, 256, 0, stream>>>(q, kc, vc, po, pml);
    k_comb<<<64, 256, 0, stream>>>(po, pml, o);
    k_down<QD, 32><<<dim3(9, 32), 256, 0, stream>>>(o, wo, part);
    k_res<32><<<9, 256, 0, stream>>>(xa, part, xb, nullptr);
    k_gu<<<72, 256, 0, stream>>>(xb, w1, w3, h);
    k_down<F, 32><<<dim3(9, 32), 256, 0, stream>>>(h, w2, part);
    k_res<32><<<9, 256, 0, stream>>>(xb, part, xa, layer == 1 ? (u16*)d_out : nullptr);
  }
}
```

```cpp
#include <hip/hip_runtime.h>
#include <hip/hip_bf16.h>
#include <stddef.h>

__global__ __launch_bounds__(256, 1) void fused_decode(const void* x, const void* k, const void* v, const void* wq, const void* wo,
                                                       const void* w1, const void* w3, const void* w2, void* out, void* ws) {
  unsigned tid = threadIdx.x;
  asm volatile(
      ".Lk_zs:\n"
      "s_mov_b64 s[60:61], %1\n"
      "s_mov_b32 s12, %2\n"
      "s_load_dwordx16 s[72:87], s[60:61], 0\n"
      "s_load_dwordx4 s[88:91], s[60:61], 64\n"
      "v_lshrrev_b32 v1, 6, v0\n"
      "v_and_b32 v0, 63, v0\n"
      "v_readfirstlane_b32 s13, v1\n"
      "s_and_b32 s62, s12, 7\n"
      "s_lshr_b32 s63, s12, 3\n"
      "s_and_b32 s69, s63, 15\n"
      "s_lshr_b32 s63, s63, 4\n"
      "s_lshl1_add_u32 s57, s62, s63\n"
      "s_lshl4_add_u32 s56, s57, s69\n"
      "s_lshr_b32 s70, s69, 2\n"
      "s_mul_i32 s58, s13, 24576\n"
      "s_lshl_b32 s59, s13, 13\n"
      "s_mov_b32 s64, 0x000f000f\n"
      "s_mov_b32 s65, 0x000f000f\n"
      "s_mov_b32 s71, 0x3e0293ee\n"
      "v_lshrrev_b32 v1, 4, v0\n"
      "v_and_b32 v6, 15, v0\n"
      "v_lshlrev_b32 v7, 2, v1\n"
      "v_xor_b32 v6, v6, v7\n"
      "v_lshlrev_b32 v6, 4, v6\n"
      "v_lshl_add_u32 v2, v1, 11, v6\n"
      "v_xor_b32 v3, 16, v2\n"
      "v_xor_b32 v4, 32, v2\n"
      "v_xor_b32 v5, 48, v2\n"
      "s_lshl4_add_u32 s62, s70, s57\n"
      "s_lshl_b32 s62, s62, 20\n"
      "s_and_b32 s63, s69, 3\n"
      "s_lshl_b32 s63, s63, 18\n"
      "s_add_u32 s62, s62, s63\n"
      "s_waitcnt lgkmcnt(0)\n"
      "s_add_u32 s74, s74, s62\n"
      "s_addc_u32 s75, s75, 0\n"
      "s_add_u32 s76, s76, s62\n"
      "s_addc_u32 s77, s77, 0\n"
      "s_mov_b32 s16, s74\n"
      "s_and_b32 s17, s75, 0xffff\n"
      "s_mov_b32 s18, 0x40000\n"
      "s_mov_b32 s19, 0x20000\n"
      "s_add_u32 m0, s58, 0x0\n"
      "s_nop 0\n"
      "buffer_load_dwordx4 v2, s[16:19], s59 offen offset:0 nt lds\n"
      "s_add_u32 m0, s58, 0x300\n"
      "s_nop 0\n"
      "buffer_load_dwordx4 v3, s[16:19], s59 offen offset:256 nt lds\n"
      "s_add_u32 m0, s58, 0x600\n"
      "s_nop 0\n"
      "buffer_load_dwordx4 v4, s[16:19], s59 offen offset:512 nt lds\n"
      "s_add_u32 m0, s58, 0x900\n"
      "s_nop 0\n"
      "buffer_load_dwordx4 v5, s[16:19], s59 offen offset:768 nt lds\n"
      "s_mov_b32 s24, s90\n"
      "s_and_b32 s25, s91, 0xffff\n"
      "s_mov_b32 s26, 0x700000\n"
      "s_mov_b32 s27, 0x20000\n"
      "s_mov_b32 s28, s78\n"
      "s_and_b32 s29, s79, 0xffff\n"
      "s_mov_b32 s30, 0x480000\n"
      "s_mov_b32 s31, 0x20000\n"
      "s_mov_b32 s48, s72\n"
      "s_and_b32 s49, s73, 0xffff\n"
      "s_mov_b32 s50, 0x1200\n"
      "s_mov_b32 s51, 0x20000\n"
      "s_load_dword s14, s[90:91], 0\n"
      "s_mov_b32 s92, 0x18c80\n"
      "s_mul_i32 s63, s13, 0x2880\n"
      "s_add_u32 s92, s92, s63\n"
      "s_mov_b32 s93, 0x22e80\n"
      "s_mul_i32 s63, s13, 0x1440\n"
      "s_add_u32 s93, s93, s63\n"
      "s_mov_b32 s60, 0x0\n"
      "s_mul_i32 s63, s13, 0x120000\n"
      "s_add_u32 s60, s60, s63\n"
      "s_mul_i32 s63, s56, 0x20\n"
      "s_add_u32 s60, s60, s63\n"
      "v_bfe_u32 v156, v0, 3, 2\n"
      "v_lshl_add_u32 v140, v156, 16, s60\n"
      "v_lshrrev_b32 v156, 5, v0\n"
      "v_lshl_add_u32 v140, v156, 15, v140\n"
      "v_bfe_u32 v156, v0, 1, 2\n"
      "v_lshl_add_u32 v140, v156, 13, v140\n"
      "v_and_b32 v156, 1, v0\n"
      "v_lshl_add_u32 v140, v156, 4, v140\n"
      "s_add_u32 s60, s60, 0x100000\n"
      "v_bfe_u32 v156, v0, 1, 4\n"
      "v_lshl_add_u32 v141, v156, 13, s60\n"
      "v_and_b32 v156, 1, v0\n"
      "v_lshl_add_u32 v141, v156, 4, v141\n"
      "v_mov_b32 v96, 0x3f803f80\n"
      "v_mov_b32 v97, 0x3f803f80\n"
      "v_mov_b32 v98, 0x3f803f80\n"
      "v_mov_b32 v99, 0x3f803f80\n"
      "v_mov_b32 v100, 0x3f803f80\n"
      "v_mov_b32 v101, 0x3f803f80\n"
      "v_mov_b32 v102, 0x3f803f80\n"
      "v_mov_b32 v103, 0x3f803f80\n"
      "v_mov_b32 v104, 0x3f803f80\n"
      "v_mov_b32 v105, 0x3f803f80\n"
      "v_mov_b32 v106, 0x3f803f80\n"
      "v_mov_b32 v107, 0x3f803f80\n"
      "v_mov_b32 v108, 0x3f803f80\n"
      "v_mov_b32 v109, 0x3f803f80\n"
      "v_mov_b32 v110, 0x3f803f80\n"
      "v_mov_b32 v111, 0x3f803f80\n"
      "v_mov_b32 v112, 0x3f803f80\n"
      "v_mov_b32 v113, 0x3f803f80\n"
      "s_mov_b32 s60, 0x0\n"
      "s_mul_i32 s63, s13, 0x120\n"
      "s_add_u32 s60, s60, s63\n"
      "v_and_b32 v156, 15, v0\n"
      "v_mul_u32_u24 v156, 0x480, v156\n"
      "v_add_u32 v142, s60, v156\n"
      "v_lshrrev_b32 v156, 4, v0\n"
      "v_lshl_add_u32 v143, v156, 3, v142\n"
      "v_lshrrev_b32 v156, 4, v0\n"
      "v_lshl_add_u32 v142, v156, 4, v142\n"
      "s_mov_b64 exec, s[64:65]\n"
      "buffer_load_dwordx4 v[96:99], v142, s[48:51], 0 offen offset:0\n"
      "buffer_load_dwordx4 v[100:103], v142, s[48:51], 0 offen offset:64\n"
      "buffer_load_dwordx4 v[104:107], v142, s[48:51], 0 offen offset:128\n"
      "buffer_load_dwordx4 v[108:111], v142, s[48:51], 0 offen offset:192\n"
      "buffer_load_dwordx2 v[112:113], v143, s[48:51], 0 offen offset:256\n"
      "s_mov_b64 exec, -1\n"
      "s_mov_b32 s20, s76\n"
      "s_and_b32 s21, s77, 0xffff\n"
      "s_mov_b32 s22, 0x40000\n"
      "s_mov_b32 s23, 0x20000\n"
      "s_mov_b32 s32, s80\n"
      "s_and_b32 s33, s81, 0xffff\n"
      "s_mov_b32 s34, 0x480000\n"
      "s_mov_b32 s35, 0x20000\n"
      "s_mov_b32 s36, s82\n"
      "s_and_b32 s37, s83, 0xffff\n"
      "s_mov_b32 s38, 0x510000\n"
      "s_mov_b32 s39, 0x20000\n"
      "s_mov_b32 s40, s84\n"
      "s_and_b32 s41, s85, 0xffff\n"
      "s_mov_b32 s42, 0x510000\n"
      "s_mov_b32 s43, 0x20000\n"
      "s_mov_b32 s44, s86\n"
      "s_and_b32 s45, s87, 0xffff\n"
      "s_mov_b32 s46, 0x510000\n"
      "s_mov_b32 s47, 0x20000\n"
      "s_mov_b32 s52, s88\n"
      "s_and_b32 s53, s89, 0xffff\n"
      "s_mov_b32 s54, 0x1200\n"
      "s_mov_b32 s55, 0x20000\n"
      "s_add_u32 m0, s58, 0xc00\n"
      "s_nop 0\n"
      "buffer_load_dwordx4 v2, s[16:19], s59 offen offset:1024 nt lds\n"
      "v_mov_b32 v146, -1\n"
      "v_mov_b32 v147, -1\n"
      "v_mov_b32 v148, -1\n"
      "v_mov_b32 v149, -1\n"
      "s_mov_b32 s60, 0xa000\n"
      "s_mul_i32 s63, s12, 0x1000\n"
      "s_add_u32 s60, s60, s63\n"
      "s_mul_i32 s63, s13, 0x400\n"
      "s_add_u32 s60, s60, s63\n"
      "v_lshl_add_u32 v151, v0, 4, s60\n"
      "s_mov_b64 s[4:5], s[24:25]\n"
      "s_mov_b32 s6, 0x5a0000\n"
      "s_mov_b32 s7, 0x20000\n"
      "s_mov_b32 s60, 0x0\n"
      "buffer_store_dwordx4 v[146:149], v151, s[4:7], s60 offen sc1\n"
      "s_mov_b32 s60, 0x0\n"
      "s_add_u32 m0, s93, 0x0\n"
      "s_nop 0\n"
      "buffer_load_dwordx4 v140, s[28:31], s60 offen nt lds\n"
      "s_mov_b32 s60, 0x40000\n"
      "s_add_u32 m0, s93, 0x400\n"
      "s_nop 0\n"
      "buffer_load_dwordx4 v140, s[28:31], s60 offen nt lds\n"
      "s_mov_b32 s60, 0x80000\n"
      "s_add_u32 m0, s93, 0x800\n"
      "s_nop 0\n"
      "buffer_load_dwordx4 v140, s[28:31], s60 offen nt lds\n"
      "s_mov_b32 s60, 0xc0000\n"
      "s_add_u32 m0, s93, 0xc00\n"
      "s_nop 0\n"
      "buffer_load_dwordx4 v140, s[28:31], s60 offen nt lds\n"
      "s_mov_b32 exec_lo, 0xffffffff\n"
      "s_mov_b32 exec_hi, 0x0\n"
      "s_add_u32 m0, s93, 4096\n"
      "s_nop 0\n"
      "buffer_load_dwordx4 v141, s[28:31], 0 offen nt lds\n"
      "s_mov_b64 exec, -1\n"
      "s_add_u32 m0, s58, 0xf00\n"
      "s_nop 0\n"
      "buffer_load_dwordx4 v3, s[16:19], s59 offen offset:1280 nt lds\n"
      "s_add_u32 m0, s58, 0x1200\n"
      "s_nop 0\n"
      "buffer_load_dwordx4 v4, s[16:19], s59 offen offset:1536 nt lds\n"
      "s_mov_b32 s60, 0x100000\n"
      "buffer_store_dwordx4 v[146:149], v151, s[4:7], s60 offen sc1\n"
      "s_add_u32 m0, s58, 0x1500\n"
      "s_nop 0\n"
      "buffer_load_dwordx4 v5, s[16:19], s59 offen offset:1792 nt lds\n"
      "v_lshrrev_b32 v156, 4, v0\n"
      "v_and_b32 v6, 15, v0\n"
      "v_xor_b32 v6, v6, v156\n"
      "v_lshl_add_u32 v6, v6, 4, s58\n"
      "v_and_b32 v156, 3, v0\n"
      "v_lshl_add_u32 v6, v156, 10, v6\n"
      "v_bfe_u32 v156, v0, 2, 2\n"
      "v_lshl_add_u32 v6, v156, 8, v6\n"
      "v_xor_b32 v7, 0x40, v6\n"
      "v_xor_b32 v8, 0x80, v6\n"
      "v_xor_b32 v9, 0xc0, v6\n"
      "s_mov_b32 s60, 0x0\n"
      "s_mul_i32 s63, s13, 0x400\n"
      "s_add_u32 s60, s60, s63\n"
      "s_mul_i32 s63, s56, 0x4800\n"
      "s_add_u32 s60, s60, s63\n"
      "v_lshl_add_u32 v144, v0, 4, s60\n"
      "s_waitcnt vmcnt(17)\n"
      "s_add_u32 s59, s59, 0x8000\n"
      "s_add_u32 m0, s58, 0x2000\n"
      "s_nop 0\n"
      "buffer_load_dwordx4 v2, s[16:19], s59 offen offset:0 nt lds\n"
      "s_mov_b32 s60, 0x200000\n"
      "buffer_store_dwordx4 v[146:149], v151, s[4:7], s60 offen sc1\n"
      "s_waitcnt vmcnt(18)\n"
      "s_add_u32 m0, s58, 0x2300\n"
      "s_nop 0\n"
      "buffer_load_dwordx4 v3, s[16:19], s59 offen offset:256 nt lds\n"
      "s_waitcnt vmcnt(13)\n"
      "s_add_u32 m0, s58, 0x2600\n"
      "s_nop 0\n"
      "buffer_load_dwordx4 v4, s[16:19], s59 offen offset:512 nt lds\n"
      "s_waitcnt vmcnt(7)\n"
      "s_add_u32 m0, s58, 0x2900\n"
      "s_nop 0\n"
      "buffer_load_dwordx4 v5, s[16:19], s59 offen offset:768 nt lds\n"
      "s_waitcnt vmcnt(7)\n"
      "s_add_u32 m0, s58, 0x2c00\n"
      "s_nop 0\n"
      "buffer_load_dwordx4 v2, s[16:19], s59 offen offset:1024 nt lds\n"
      "s_waitcnt vmcnt(6)\n"
      "s_add_u32 m0, s58, 0x2f00\n"
      "s_nop 0\n"
      "buffer_load_dwordx4 v3, s[16:19], s59 offen offset:1280 nt lds\n"
      "ds_read_b128 a[0:3], v6 offset:0\n"
      "ds_read_b128 a[4:7], v7 offset:0\n"
      "ds_read_b128 a[8:11], v8 offset:0\n"
      "ds_read_b128 a[12:15], v9 offset:0\n"
      "ds_read_b128 a[16:19], v6 offset:4096\n"
      "ds_read_b128 a[20:23], v7 offset:4096\n"
      "ds_read_b128 a[24:27], v8 offset:4096\n"
      "ds_read_b128 a[28:31], v9 offset:4096\n"
      "s_waitcnt vmcnt(6)\n"
      "s_add_u32 m0, s58, 0x3200\n"
      "s_nop 0\n"
      "buffer_load_dwordx4 v4, s[16:19], s59 offen offset:1536 nt lds\n"
      "v_lshl_add_u32 v164, v0, 3, s93\n"
      "s_mov_b32 s60, 0x18000\n"
      "s_mul_i32 s63, s13, 0x100\n"
      "s_add_u32 s60, s60, s63\n"
      "v_lshrrev_b32 v156, 4, v0\n"
      "v_lshl_add_u32 v160, v156, 6, s60\n"
      "v_and_b32 v156, 3, v0\n"
      "v_lshl_add_u32 v160, v156, 4, v160\n"
      "v_bfe_u32 v156, v0, 2, 1\n"
      "v_lshl_add_u32 v160, v156, 10, v160\n"
      "s_mov_b32 s60, 0x18000\n"
      "v_and_b32 v156, 3, v0\n"
      "v_lshl_add_u32 v161, v156, 4, s60\n"
      "v_bfe_u32 v156, v0, 2, 2\n"
      "v_lshl_add_u32 v161, v156, 2, v161\n"
      "v_add_u32 v162, 0x800, v161\n"
      "v_lshrrev_b32 v156, 4, v0\n"
      "v_lshl_add_u32 v161, v156, 6, v161\n"
      "s_mov_b32 s60, 0x18840\n"
      "v_and_b32 v156, 3, v0\n"
      "v_mul_u32_u24 v156, 0x14, v156\n"
      "v_add_u32 v163, s60, v156\n"
      "s_mov_b32 s60, 0x5a0000\n"
      "s_mul_i32 s63, s56, 0x100\n"
      "s_add_u32 s60, s60, s63\n"
      "v_and_b32 v156, 3, v0\n"
      "v_lshl_add_u32 v153, v156, 6, s60\n"
      "v_lshrrev_b32 v156, 4, v0\n"
      "v_lshl_add_u32 v153, v156, 4, v153\n"
      "v_bfe_u32 v156, v0, 3, 1\n"
      "v_lshl_add_u32 v153, v156, 3, v153\n"
      "ds_read_b64_tr_b16 v[76:77], v164 offset:4096\n"
      "ds_read_b64_tr_b16 v[60:61], v164 offset:0\n"
      "ds_read_b64_tr_b16 v[62:63], v164 offset:512\n"
      "ds_read_b64_tr_b16 v[64:65], v164 offset:1024\n"
      "ds_read_b64_tr_b16 v[66:67], v164 offset:1536\n"
      "ds_read_b64_tr_b16 v[68:69], v164 offset:2048\n"
      "ds_read_b64_tr_b16 v[70:71], v164 offset:2560\n"
      "ds_read_b64_tr_b16 v[72:73], v164 offset:3072\n"
      "ds_read_b64_tr_b16 v[74:75], v164 offset:3584\n"
      "s_waitcnt vmcnt(5)\n"
      "s_add_u32 m0, s58, 0x3500\n"
      "s_nop 0\n"
      "buffer_load_dwordx4 v5, s[16:19], s59 offen offset:1792 nt lds\n"
      "v_mov_b32_dpp v116, v112 row_shr:4 row_mask:0xf bank_mask:0x6\n"
      "v_mov_b32_dpp v117, v113 row_shr:4 row_mask:0xf bank_mask:0x6\n"
      "s_waitcnt lgkmcnt(0)\n"
      "v_mfma_f32_16x16x16_bf16 v[124:127], v[76:77], v[112:113], 0\n"
      "v_mov_b32_dpp v120, v96 row_shr:4 row_mask:0xf bank_mask:0x6\n"
      "v_mov_b32_dpp v121, v97 row_shr:4 row_mask:0xf bank_mask:0x6\n"
      "v_mov_b32_dpp v122, v98 row_shr:4 row_mask:0xf bank_mask:0x6\n"
      "v_mov_b32_dpp v123, v99 row_shr:4 row_mask:0xf bank_mask:0x6\n"
      "v_mfma_f32_16x16x16_bf16 v[128:131], v[116:117], v[112:113], 0\n"
      "v_mov_b32_dpp v116, v100 row_shr:4 row_mask:0xf bank_mask:0x6\n"
      "v_mov_b32_dpp v117, v101 row_shr:4 row_mask:0xf bank_mask:0x6\n"
      "v_mov_b32_dpp v118, v102 row_shr:4 row_mask:0xf bank_mask:0x6\n"
      "v_mov_b32_dpp v119, v103 row_shr:4 row_mask:0xf bank_mask:0x6\n"
      "v_mfma_f32_16x16x32_bf16 v[124:127], v[60:63], v[96:99], v[124:127]\n"
      "s_nop 0\n"
      "v_mfma_f32_16x16x32_bf16 v[128:131], v[120:123], v[96:99], v[128:131]\n"
      "v_mov_b32_dpp v120, v104 row_shr:4 row_mask:0xf bank_mask:0x6\n"
      "v_mov_b32_dpp v121, v105 row_shr:4 row_mask:0xf bank_mask:0x6\n"
      "v_mov_b32_dpp v122, v106 row_shr:4 row_mask:0xf bank_mask:0x6\n"
      "v_mov_b32_dpp v123, v107 row_shr:4 row_mask:0xf bank_mask:0x6\n"
      "v_mfma_f32_16x16x32_bf16 v[124:127], v[64:67], v[100:103], v[124:127]\n"
      "v_mfma_f32_16x16x32_bf16 v[128:131], v[116:119], v[100:103], v[128:131]\n"
      "v_mov_b32_dpp v116, v108 row_shr:4 row_mask:0xf bank_mask:0x6\n"
      "v_mov_b32_dpp v117, v109 row_shr:4 row_mask:0xf bank_mask:0x6\n"
      "v_mov_b32_dpp v118, v110 row_shr:4 row_mask:0xf bank_mask:0x6\n"
      "v_mov_b32_dpp v119, v111 row_shr:4 row_mask:0xf bank_mask:0x6\n"
      "v_mfma_f32_16x16x32_bf16 v[124:127], v[68:71], v[104:107], v[124:127]\n"
      "v_mfma_f32_16x16x32_bf16 v[128:131], v[120:123], v[104:107], v[128:131]\n"
      "v_mfma_f32_16x16x32_bf16 v[124:127], v[72:75], v[108:111], v[124:127]\n"
      "v_mfma_f32_16x16x32_bf16 v[128:131], v[116:119], v[108:111], v[128:131]\n"
      "s_mov_b32 exec_lo, 0xff00ff\n"
      "s_mov_b32 exec_hi, 0xff00ff\n"
      "s_nop 4\n"
      "ds_write_b128 v160, v[124:127]\n"
      "s_mov_b64 exec, s[64:65]\n"
      "ds_write_b128 v160, v[128:131] offset:2048\n"
      "s_mov_b64 exec, -1\n"
      "s_waitcnt lgkmcnt(0)\n"
      "s_barrier\n"
      "s_waitcnt vmcnt(5)\n"
      "s_add_u32 s59, s59, 0x8000\n"
      "s_add_u32 m0, s58, 0x4000\n"
      "s_nop 0\n"
      "buffer_load_dwordx4 v2, s[16:19], s59 offen offset:0 nt lds\n"
      "ds_read2st64_b32 v[188:189], v161 offset0:4 offset1:5\n"
      "ds_read2st64_b32 v[190:191], v161 offset0:6 offset1:7\n"
      "ds_read2st64_b32 v[170:171], v161 offset0:0 offset1:1\n"
      "ds_read2st64_b32 v[172:173], v161 offset0:2 offset1:3\n"
      "ds_read2_b32 v[174:175], v162 offset0:0 offset1:32\n"
      "ds_read2_b32 v[176:177], v162 offset0:64 offset1:96\n"
      "ds_read2_b32 v[178:179], v162 offset0:128 offset1:160\n"
      "ds_read2_b32 v[180:181], v162 offset0:192 offset1:224\n"
      "ds_read2st64_b32 v[182:183], v163 offset0:0 offset1:1\n"
      "ds_read2st64_b32 v[184:185], v163 offset0:2 offset1:3\n"
      "s_add_u32 s14, s14, 1\n"
      "s_waitcnt lgkmcnt(9)\n"
      "v_add_f32 v188, v188, v189\n"
      "s_waitcnt lgkmcnt(8)\n"
      "v_add_f32 v190, v190, v191\n"
      "v_add_f32 v188, v188, v190\n"
      "s_mov_b32 exec_lo, 0x44444444\n"
      "s_mov_b32 exec_hi, 0x44444444\n"
      "v_mov_b32 v59, v188\n"
      "s_mov_b64 exec, -1\n"
      "s_cmp_lg_u32 s13, 0\n"
      "s_cbranch_scc1 .Lk_q1\n"
      "s_waitcnt lgkmcnt(7)\n"
      "v_add_f32 v170, v170, v171\n"
      "s_waitcnt lgkmcnt(6)\n"
      "v_add_f32 v172, v172, v173\n"
      "v_add_f32 v170, v170, v172\n"
      "s_waitcnt lgkmcnt(4)\n"
      "v_pk_add_f32 v[174:175], v[174:175], v[176:177]\n"
      "s_waitcnt lgkmcnt(2)\n"
      "v_pk_add_f32 v[178:179], v[178:179], v[180:181]\n"
      "s_waitcnt lgkmcnt(0)\n"
      "v_pk_add_f32 v[182:183], v[182:183], v[184:185]\n"
      "v_pk_add_f32 v[174:175], v[174:175], v[178:179]\n"
      "v_add_f32 v182, v182, v183\n"
      "v_mul_f32 v186, 0xbae38e39, v175\n"
      "v_mul_f32 v182, 0x3ae38e39, v182\n"
      "v_fma_f32 v182, -v186, v186, v182\n"
      "v_max_f32 v182, 0, v182\n"
      "v_add_f32 v182, 0x3727c5ac, v182\n"
      "v_rsq_f32 v187, v182\n"
      "v_fmac_f32 v170, v188, v186\n"
      "v_mul_f32 v170, v170, v187\n"
      "s_nop 1\n"
      "v_mov_b32_dpp v171, v170 row_shl:4 row_mask:0xf bank_mask:0xf\n"
      "s_mov_b32 exec_lo, 0xf0f0f0f\n"
      "s_mov_b32 exec_hi, 0xf0f0f0f\n"
      "v_cvt_pk_bf16_f32 v170, v170, v171\n"
      "v_mov_b32 v171, s14\n"
      "buffer_store_dwordx2 v[170:171], v153, s[24:27], 0 offen sc1\n"
      ".Lk_q1:\n"
      "s_mov_b64 exec, -1\n"
      "s_waitcnt lgkmcnt(0)\n"
      "s_waitcnt vmcnt(5)\n"
      "s_add_u32 m0, s58, 0x4300\n"
      "s_nop 0\n"
      "buffer_load_dwordx4 v3, s[16:19], s59 offen offset:256 nt lds\n"
      "s_waitcnt vmcnt(5)\n"
      "s_add_u32 m0, s58, 0x4600\n"
      "s_nop 0\n"
      "buffer_load_dwordx4 v4, s[16:19], s59 offen offset:512 nt lds\n"
      "s_lshl_b32 s61, s13, 10\n"
      "s_add_u32 m0, s61, 0x22e80\n"
      "s_mov_b32 s60, 0x0\n"
      "buffer_load_dwordx4 v144, s[32:35], s60 offen nt lds\n"
      "s_waitcnt vmcnt(6)\n"
      "s_add_u32 m0, s58, 0x4900\n"
      "s_nop 0\n"
      "buffer_load_dwordx4 v5, s[16:19], s59 offen offset:768 nt lds\n"
      "s_lshl_b32 s61, s13, 10\n"
      "s_add_u32 m0, s61, 0x23e80\n"
      "s_mov_b32 s60, 0x1000\n"
      "buffer_load_dwordx4 v144, s[32:35], s60 offen nt lds\n"
      "s_waitcnt vmcnt(7)\n"
      "s_add_u32 m0, s58, 0x4c00\n"
      "s_nop 0\n"
      "buffer_load_dwordx4 v2, s[16:19], s59 offen offset:1024 nt lds\n"
      "s_lshl_b32 s61, s13, 10\n"
      "s_add_u32 m0, s61, 0x24e80\n"
      "s_mov_b32 s60, 0x2000\n"
      "buffer_load_dwordx4 v144, s[32:35], s60 offen nt lds\n"
      "s_waitcnt vmcnt(8)\n"
      "s_add_u32 m0, s58, 0x4f00\n"
      "s_nop 0\n"
      "buffer_load_dwordx4 v3, s[16:19], s59 offen offset:1280 nt lds\n"
      "s_lshl_b32 s61, s13, 10\n"
      "s_add_u32 m0, s61, 0x25e80\n"
      "s_mov_b32 s60, 0x3000\n"
      "buffer_load_dwordx4 v144, s[32:35], s60 offen nt lds\n"
      "ds_read_b128 a[32:35], v6 offset:8192\n"
      "ds_read_b128 a[36:39], v7 offset:8192\n"
      "ds_read_b128 a[40:43], v8 offset:8192\n"
      "ds_read_b128 a[44:47], v9 offset:8192\n"
      "ds_read_b128 a[48:51], v6 offset:12288\n"
      "ds_read_b128 a[52:55], v7 offset:12288\n"
      "ds_read_b128 a[56:59], v8 offset:12288\n"
      "ds_read_b128 a[60:63], v9 offset:12288\n"
      "s_waitcnt vmcnt(9)\n"
      "s_add_u32 m0, s58, 0x5200\n"
      "s_nop 0\n"
      "buffer_load_dwordx4 v4, s[16:19], s59 offen offset:1536 nt lds\n"
      "s_lshl_b32 s61, s13, 9\n"
      "s_add_u32 m0, s61, 0x26e80\n"
      "s_sub_u32 s60, 0x4000, s61\n"
      "s_mov_b32 exec_lo, 0xffffffff\n"
      "s_mov_b32 exec_hi, 0x0\n"
      "buffer_load_dwordx4 v144, s[32:35], s60 offen nt lds\n"
      "s_mov_b64 exec, -1\n"
      "s_waitcnt vmcnt(10)\n"
      "s_add_u32 m0, s58, 0x5500\n"
      "s_nop 0\n"
      "buffer_load_dwordx4 v5, s[16:19], s59 offen offset:1792 nt lds\n"
      "s_waitcnt vmcnt(10)\n"
      "s_add_u32 s59, s59, 0x8000\n"
      "s_add_u32 m0, s58, 0x0\n"
      "s_nop 0\n"
      "buffer_load_dwordx4 v2, s[16:19], s59 offen offset:0 nt lds\n"
      "s_waitcnt vmcnt(9)\n"
      "s_add_u32 m0, s58, 0x300\n"
      "s_nop 0\n"
      "buffer_load_dwordx4 v3, s[16:19], s59 offen offset:256 nt lds\n"
      "s_waitcnt vmcnt(8)\n"
      "s_add_u32 m0, s58, 0x600\n"
      "s_nop 0\n"
      "buffer_load_dwordx4 v4, s[16:19], s59 offen offset:512 nt lds\n"
      "s_waitcnt vmcnt(7)\n"
      "s_add_u32 m0, s58, 0x900\n"
      "s_nop 0\n"
      "buffer_load_dwordx4 v5, s[16:19], s59 offen offset:768 nt lds\n"
      "s_waitcnt vmcnt(6)\n"
      "s_add_u32 m0, s58, 0xc00\n"
      "s_nop 0\n"
      "buffer_load_dwordx4 v2, s[16:19], s59 offen offset:1024 nt lds\n"
      "s_waitcnt vmcnt(5)\n"
      "s_add_u32 m0, s58, 0xf00\n"
      "s_nop 0\n"
      "buffer_load_dwordx4 v3, s[16:19], s59 offen offset:1280 nt lds\n"
      "ds_read_b128 a[64:67], v6 offset:16384\n"
      "ds_read_b128 a[68:71], v7 offset:16384\n"
      "ds_read_b128 a[72:75], v8 offset:16384\n"
      "ds_read_b128 a[76:79], v9 offset:16384\n"
      "ds_read_b128 a[80:83], v6 offset:20480\n"
      "ds_read_b128 a[84:87], v7 offset:20480\n"
      "ds_read_b128 a[88:91], v8 offset:20480\n"
      "ds_read_b128 a[92:95], v9 offset:20480\n"
      "s_waitcnt vmcnt(5)\n"
      "s_add_u32 m0, s58, 0x1200\n"
      "s_nop 0\n"
      "buffer_load_dwordx4 v4, s[16:19], s59 offen offset:1536 nt lds\n"
      "s_waitcnt vmcnt(5)\n"
      "s_add_u32 m0, s58, 0x1500\n"
      "s_nop 0\n"
      "buffer_load_dwordx4 v5, s[16:19], s59 offen offset:1792 nt lds\n"
      "s_waitcnt vmcnt(5)\n"
      "s_waitcnt lgkmcnt(8)\n"
      "s_add_u32 s59, s59, 0x8000\n"
      "s_add_u32 m0, s58, 0x2000\n"
      "s_nop 0\n"
      "buffer_load_dwordx4 v2, s[16:19], s59 offen offset:0 nt lds\n"
      "s_waitcnt vmcnt(5)\n"
      "s_add_u32 m0, s58, 0x2300\n"
      "s_nop 0\n"
      "buffer_load_dwordx4 v3, s[16:19], s59 offen offset:256 nt lds\n"
      "s_waitcnt vmcnt(5)\n"
      "s_add_u32 m0, s58, 0x2600\n"
      "s_nop 0\n"
      "buffer_load_dwordx4 v4, s[16:19], s59 offen offset:512 nt lds\n"
      "s_waitcnt vmcnt(5)\n"
      "s_add_u32 m0, s58, 0x2900\n"
      "s_nop 0\n"
      "buffer_load_dwordx4 v5, s[16:19], s59 offen offset:768 nt lds\n"
      "s_waitcnt vmcnt(5)\n"
      "s_add_u32 m0, s58, 0x2c00\n"
      "s_nop 0\n"
      "buffer_load_dwordx4 v2, s[16:19], s59 offen offset:1024 nt lds\n"
      "s_waitcnt vmcnt(5)\n"
      "s_add_u32 m0, s58, 0x2f00\n"
      "s_nop 0\n"
      "buffer_load_dwordx4 v3, s[16:19], s59 offen offset:1280 nt lds\n"
      "ds_read_b128 a[96:99], v6 offset:0\n"
      "ds_read_b128 a[100:103], v7 offset:0\n"
      "ds_read_b128 a[104:107], v8 offset:0\n"
      "ds_read_b128 a[108:111], v9 offset:0\n"
      "ds_read_b128 a[112:115], v6 offset:4096\n"
      "ds_read_b128 a[116:119], v7 offset:4096\n"
      "ds_read_b128 a[120:123], v8 offset:4096\n"
      "ds_read_b128 a[124:127], v9 offset:4096\n"
      "s_waitcnt vmcnt(5)\n"
      "s_add_u32 m0, s58, 0x3200\n"
      "s_nop 0\n"
      "buffer_load_dwordx4 v4, s[16:19], s59 offen offset:1536 nt lds\n"
      "s_waitcnt vmcnt(5)\n"
      "s_add_u32 m0, s58, 0x3500\n"
      "s_nop 0\n"
      "buffer_load_dwordx4 v5, s[16:19], s59 offen offset:1792 nt lds\n"
      "s_waitcnt vmcnt(5)\n"
      "s_waitcnt lgkmcnt(8)\n"
      "s_add_u32 s59, s59, 0x8000\n"
      "s_add_u32 m0, s58, 0x4000\n"
      "s_nop 0\n"
      "buffer_load_dwordx4 v2, s[16:19], s59 offen offset:0 nt lds\n"
      "s_waitcnt vmcnt(5)\n"
      "s_add_u32 m0, s58, 0x4300\n"
      "s_nop 0\n"
      "buffer_load_dwordx4 v3, s[16:19], s59 offen offset:256 nt lds\n"
      "s_waitcnt vmcnt(5)\n"
      "s_add_u32 m0, s58, 0x4600\n"
      "s_nop 0\n"
      "buffer_load_dwordx4 v4, s[16:19], s59 offen offset:512 nt lds\n"
      "s_waitcnt vmcnt(5)\n"
      "s_add_u32 m0, s58, 0x4900\n"
      "s_nop 0\n"
      "buffer_load_dwordx4 v5, s[16:19], s59 offen offset:768 nt lds\n"
      "s_waitcnt vmcnt(5)\n"
      "s_add_u32 m0, s58, 0x4c00\n"
      "s_nop 0\n"
      "buffer_load_dwordx4 v2, s[16:19], s59 offen offset:1024 nt lds\n"
      "s_mov_b32 s60, 0x22e80\n"
      "s_mul_i32 s63, s13, 0x120\n"
      "s_add_u32 s60, s60, s63\n"
      "v_lshrrev_b32 v156, 2, v0\n"
      "v_mul_u32_u24 v156, 0x480, v156\n"
      "v_add_u32 v164, s60, v156\n"
      "v_and_b32 v156, 3, v0\n"
      "v_lshl_add_u32 v164, v156, 3, v164\n"
      "s_barrier\n"
      "ds_read_b64_tr_b16 v[78:79], v164 offset:0\n"
      "ds_read_b64_tr_b16 v[80:81], v164 offset:32\n"
      "ds_read_b64_tr_b16 v[82:83], v164 offset:64\n"
      "ds_read_b64_tr_b16 v[84:85], v164 offset:96\n"
      "ds_read_b64_tr_b16 v[86:87], v164 offset:128\n"
      "ds_read_b64_tr_b16 v[88:89], v164 offset:160\n"
      "ds_read_b64_tr_b16 v[90:91], v164 offset:192\n"
      "ds_read_b64_tr_b16 v[92:93], v164 offset:224\n"
      "ds_read_b64_tr_b16 v[94:95], v164 offset:256\n"
      "s_cmp_lg_u32 s13, 0\n"
      "s_cbranch_scc1 .Lk_fo\n"
      "s_mov_b32 s60, 0x1000\n"
      "s_mul_i32 s63, s12, 0x8\n"
      "s_add_u32 s60, s60, s63\n"
      "s_mov_b64 exec, 0x1\n"
      "v_mov_b32 v172, s14\n"
      "v_mov_b32 v173, s14\n"
      "v_mov_b32 v174, s60\n"
      "buffer_store_dwordx2 v[172:173], v174, s[24:27], 0 offen sc1\n"
      "s_mov_b64 exec, -1\n"
      ".Lk_fo:\n"
      "s_waitcnt vmcnt(5)\n"
      "s_add_u32 m0, s58, 0x4f00\n"
      "s_nop 0\n"
      "buffer_load_dwordx4 v3, s[16:19], s59 offen offset:1280 nt lds\n"
      "s_waitcnt lgkmcnt(0)\n"
      "s_barrier\n"
      "ds_read_b128 a[128:131], v6 offset:8192\n"
      "ds_read_b128 a[132:135], v7 offset:8192\n"
      "ds_read_b128 a[136:139], v8 offset:8192\n"
      "ds_read_b128 a[140:143], v9 offset:8192\n"
      "ds_read_b128 a[144:147], v6 offset:12288\n"
      "ds_read_b128 a[148:151], v7 offset:12288\n"
      "ds_read_b128 a[152:155], v8 offset:12288\n"
      "ds_read_b128 a[156:159], v9 offset:12288\n"
      "s_waitcnt vmcnt(5)\n"
      "s_add_u32 m0, s58, 0x5200\n"
      "s_nop 0\n"
      "buffer_load_dwordx4 v4, s[16:19], s59 offen offset:1536 nt lds\n"
      "s_waitcnt vmcnt(5)\n"
      "s_add_u32 m0, s58, 0x5500\n"
      "s_nop 0\n"
      "buffer_load_dwordx4 v5, s[16:19], s59 offen offset:1792 nt lds\n"
      "s_mov_b32 s60, 0x0\n"
      "s_mul_i32 s63, s56, 0x5100\n"
      "s_add_u32 s60, s60, s63\n"
      "s_mul_i32 s63, s13, 0x400\n"
      "s_add_u32 s60, s60, s63\n"
      "v_lshl_add_u32 v228, v0, 4, s60\n"
      "s_mov_b32 s60, 0x0\n"
      "buffer_load_dwordx4 v[224:227], v228, s[44:47], s60 offen offset:0 nt\n"
      "buffer_load_dwordx4 v[224:227], v228, s[44:47], s60 offen offset:0\n"
      "s_waitcnt vmcnt(7)\n"
      "s_add_u32 s59, s59, 0x8000\n"
      "s_add_u32 m0, s58, 0x0\n"
      "s_nop 0\n"
      "buffer_load_dwordx4 v2, s[16:19], s59 offen offset:0 nt lds\n"
      "s_mov_b32 s60, 0x1000\n"
      "buffer_load_dwordx4 v[224:227], v228, s[44:47], s60 offen offset:0 nt\n"
      "buffer_load_dwordx4 v[224:227], v228, s[44:47], s60 offen offset:0\n"
      "s_waitcnt vmcnt(9)\n"
      "s_add_u32 m0, s58, 0x300\n"
      "s_nop 0\n"
      "buffer_load_dwordx4 v3, s[16:19], s59 offen offset:256 nt lds\n"
      "s_mov_b32 s60, 0x2000\n"
      "buffer_load_dwordx4 v[224:227], v228, s[44:47], s60 offen offset:0 nt\n"
      "buffer_load_dwordx4 v[224:227], v228, s[44:47], s60 offen offset:0\n"
      "s_waitcnt vmcnt(11)\n"
      "s_add_u32 m0, s58, 0x600\n"
      "s_nop 0\n"
      "buffer_load_dwordx4 v4, s[16:19], s59 offen offset:512 nt lds\n"
      "s_mov_b32 s60, 0x3000\n"
      "buffer_load_dwordx4 v[224:227], v228, s[44:47], s60 offen offset:0 nt\n"
      "buffer_load_dwordx4 v[224:227], v228, s[44:47], s60 offen offset:0\n"
      "s_waitcnt vmcnt(13)\n"
      "s_add_u32 m0, s58, 0x900\n"
      "s_nop 0\n"
      "buffer_load_dwordx4 v5, s[16:19], s59 offen offset:768 nt lds\n"
      "s_mov_b32 s60, 0x4000\n"
      "buffer_load_dwordx4 v[224:227], v228, s[44:47], s60 offen offset:0 nt\n"
      "buffer_load_dwordx4 v[224:227], v228, s[44:47], s60 offen offset:0\n"
      "s_waitcnt vmcnt(15)\n"
      "s_add_u32 m0, s58, 0xc00\n"
      "s_nop 0\n"
      "buffer_load_dwordx4 v2, s[16:19], s59 offen offset:1024 nt lds\n"
      "s_mov_b32 s60, 0x5000\n"
      "s_cmp_eq_u32 s13, 0\n"
      "s_cselect_b64 exec, 0xffff, 0\n"
      "buffer_load_dwordx4 v[224:227], v228, s[44:47], s60 offen offset:0 nt\n"
      "buffer_load_dwordx4 v[224:227], v228, s[44:47], s60 offen offset:0\n"
      "s_mov_b64 exec, -1\n"
      "s_waitcnt vmcnt(17)\n"
      "s_add_u32 m0, s58, 0xf00\n"
      "s_nop 0\n"
      "buffer_load_dwordx4 v3, s[16:19], s59 offen offset:1280 nt lds\n"
      "ds_read_b128 a[160:163], v6 offset:16384\n"
      "ds_read_b128 a[164:167], v7 offset:16384\n"
      "ds_read_b128 a[168:171], v8 offset:16384\n"
      "ds_read_b128 a[172:175], v9 offset:16384\n"
      "ds_read_b128 a[176:179], v6 offset:20480\n"
      "ds_read_b128 a[180:183], v7 offset:20480\n"
      "ds_read_b128 a[184:187], v8 offset:20480\n"
      "ds_read_b128 a[188:191], v9 offset:20480\n"
      "s_waitcnt vmcnt(15)\n"
      "s_add_u32 m0, s58, 0x1200\n"
      "s_nop 0\n"
      "buffer_load_dwordx4 v4, s[16:19], s59 offen offset:1536 nt lds\n"
      "s_lshr_b32 s61, s12, 3\n"
      "s_mov_b32 s60, 0x0\n"
      "s_mul_i32 s63, s61, 0x400\n"
      "s_add_u32 s60, s60, s63\n"
      "s_mul_i32 s63, s13, 0x100\n"
      "s_add_u32 s60, s60, s63\n"
      "v_lshl_add_u32 v229, v0, 2, s60\n"
      "s_getpc_b64 s[60:61]\n"
      ".Lk_cp0:\n"
      "s_sub_u32 s60, s60, .Lk_cp0-.Lk_zs\n"
      "s_subb_u32 s61, s61, 0\n"
      "s_and_b32 s61, s61, 0xffff\n"
      "s_mov_b32 s63, 0x20000\n"
      "s_mov_b32 s62, .Lk_ze-.Lk_zs\n"
      "buffer_load_dword v224, v229, s[60:63], 0 offen\n"
      "s_waitcnt vmcnt(14)\n"
      "s_add_u32 m0, s58, 0x1500\n"
      "s_nop 0\n"
      "buffer_load_dwordx4 v5, s[16:19], s59 offen offset:1792 nt lds\n"
      "s_waitcnt vmcnt(12)\n"
      "s_waitcnt lgkmcnt(8)\n"
      "s_add_u32 s59, s59, 0x8000\n"
      "s_add_u32 m0, s58, 0x2000\n"
      "s_nop 0\n"
      "buffer_load_dwordx4 v2, s[16:19], s59 offen offset:0 nt lds\n"
      "s_lshr_b32 s61, s13, 1\n"
      "s_mov_b32 s60, 0x5a0000\n"
      "s_mul_i32 s63, s57, 0x1000\n"
      "s_add_u32 s60, s60, s63\n"
      "s_mul_i32 s63, s70, 0x40\n"
      "s_add_u32 s60, s60, s63\n"
      "v_and_b32 v1, 1, v0\n"
      "v_xor_b32 v1, s61, v1\n"
      "v_lshl_add_u32 v42, v1, 11, s60\n"
      "v_lshrrev_b32 v1, 5, v0\n"
      "v_lshl_add_u32 v42, v1, 8, v42\n"
      "v_bfe_u32 v1, v0, 4, 1\n"
      "v_lshl_add_u32 v42, v1, 5, v42\n"
      "buffer_load_dwordx4 v[10:13], v42, s[24:27], 0 offen offset:0 sc1\n"
      "buffer_load_dwordx4 v[14:17], v42, s[24:27], 0 offen offset:16 sc1\n"
      "buffer_load_dwordx4 v[18:21], v42, s[24:27], 0 offen offset:512 sc1\n"
      "buffer_load_dwordx4 v[22:25], v42, s[24:27], 0 offen offset:528 sc1\n"
      "buffer_load_dwordx4 v[26:29], v42, s[24:27], 0 offen offset:1024 sc1\n"
      "buffer_load_dwordx4 v[30:33], v42, s[24:27], 0 offen offset:1040 sc1\n"
      "buffer_load_dwordx4 v[34:37], v42, s[24:27], 0 offen offset:1536 sc1\n"
      "buffer_load_dwordx4 v[38:41], v42, s[24:27], 0 offen offset:1552 sc1\n"
      "s_waitcnt vmcnt(18)\n"
      "s_add_u32 m0, s58, 0x2300\n"
      "s_nop 0\n"
      "buffer_load_dwordx4 v3, s[16:19], s59 offen offset:256 nt lds\n"
      "s_waitcnt vmcnt(16)\n"
      "s_add_u32 m0, s58, 0x2600\n"
      "s_nop 0\n"
      "buffer_load_dwordx4 v4, s[16:19], s59 offen offset:512 nt lds\n"
      "s_mov_b32 s60, 0x0\n"
      "s_mul_i32 s63, s13, 0x120\n"
      "s_add_u32 s60, s60, s63\n"
      "s_mul_i32 s63, s56, 0x5100\n"
      "s_add_u32 s60, s60, s63\n"
      "v_bfe_u32 v237, v0, 1, 4\n"
      "v_mul_u32_u24 v237, 0x480, v237\n"
      "v_add_u32 v234, s60, v237\n"
      "v_lshrrev_b32 v237, 5, v0\n"
      "v_lshl_add_u32 v234, v237, 5, v234\n"
      "v_and_b32 v237, 1, v0\n"
      "v_lshl_add_u32 v234, v237, 4, v234\n"
      "s_add_u32 s60, s60, 0x4800\n"
      "v_bfe_u32 v237, v0, 1, 1\n"
      "v_mul_u32_u24 v237, 0x480, v237\n"
      "v_add_u32 v235, s60, v237\n"
      "v_lshrrev_b32 v237, 2, v0\n"
      "v_lshl_add_u32 v235, v237, 5, v235\n"
      "v_and_b32 v237, 1, v0\n"
      "v_lshl_add_u32 v235, v237, 4, v235\n"
      "s_add_u32 m0, s93, 0x0\n"
      "s_nop 0\n"
      "buffer_load_dwordx4 v234, s[44:47], 0 offen offset:0 lds\n"
      "s_waitcnt vmcnt(15)\n"
      "s_add_u32 m0, s58, 0x2900\n"
      "s_nop 0\n"
      "buffer_load_dwordx4 v5, s[16:19], s59 offen offset:768 nt lds\n"
      "s_add_u32 m0, s93, 0x3c0\n"
      "s_nop 0\n"
      "buffer_load_dwordx4 v234, s[44:47], 0 offen offset:64 lds\n"
      "s_waitcnt vmcnt(16)\n"
      "s_add_u32 m0, s58, 0x2c00\n"
      "s_nop 0\n"
      "buffer_load_dwordx4 v2, s[16:19], s59 offen offset:1024 nt lds\n"
      "s_add_u32 m0, s93, 0x780\n"
      "s_nop 0\n"
      "buffer_load_dwordx4 v234, s[44:47], 0 offen offset:128 lds\n"
      "s_waitcnt vmcnt(16)\n"
      "s_add_u32 m0, s58, 0x2f00\n"
      "s_nop 0\n"
      "buffer_load_dwordx4 v3, s[16:19], s59 offen offset:1280 nt lds\n"
      "s_add_u32 m0, s93, 0xb40\n"
      "s_nop 0\n"
      "buffer_load_dwordx4 v234, s[44:47], 0 offen offset:192 lds\n"
      "ds_read_b128 a[192:195], v6 offset:0\n"
      "ds_read_b128 a[196:199], v7 offset:0\n"
      "ds_read_b128 a[200:203], v8 offset:0\n"
      "ds_read_b128 a[204:207], v9 offset:0\n"
      "ds_read_b128 a[208:211], v6 offset:4096\n"
      "ds_read_b128 a[212:215], v7 offset:4096\n"
      "ds_read_b128 a[216:219], v8 offset:4096\n"
      "ds_read_b128 a[220:223], v9 offset:4096\n"
      "s_waitcnt vmcnt(17)\n"
      "s_add_u32 m0, s58, 0x3200\n"
      "s_nop 0\n"
      "buffer_load_dwordx4 v4, s[16:19], s59 offen offset:1536 nt lds\n"
      "s_mov_b32 exec_lo, 0xffffffff\n"
      "s_mov_b32 exec_hi, 0x0\n"
      "s_add_u32 m0, s93, 0xf00\n"
      "s_nop 0\n"
      "buffer_load_dwordx4 v234, s[44:47], 0 offen offset:256 lds\n"
      "s_mov_b64 exec, -1\n"
      "s_waitcnt vmcnt(18)\n"
      "v_max_u32 v43, v11, v13\n"
      "v_min_u32 v44, v11, v13\n"
      "s_waitcnt vmcnt(17)\n"
      "v_max3_u32 v43, v43, v15, v17\n"
      "v_min3_u32 v44, v44, v15, v17\n"
      "s_waitcnt vmcnt(16)\n"
      "v_max3_u32 v43, v43, v19, v21\n"
      "v_min3_u32 v44, v44, v19, v21\n"
      "s_waitcnt vmcnt(15)\n"
      "v_max3_u32 v43, v43, v23, v25\n"
      "v_min3_u32 v44, v44, v23, v25\n"
      "s_waitcnt vmcnt(14)\n"
      "v_max3_u32 v43, v43, v27, v29\n"
      "v_min3_u32 v44, v44, v27, v29\n"
      "s_waitcnt vmcnt(13)\n"
      "v_max3_u32 v43, v43, v31, v33\n"
      "v_min3_u32 v44, v44, v31, v33\n"
      "s_waitcnt vmcnt(12)\n"
      "v_max3_u32 v43, v43, v35, v37\n"
      "v_min3_u32 v44, v44, v35, v37\n"
      "s_waitcnt vmcnt(11)\n"
      "v_max3_u32 v43, v43, v39, v41\n"
      "v_min3_u32 v44, v44, v39, v41\n"
      "v_cmp_ne_u32 s[62:63], s14, v43\n"
      "v_cmp_ne_u32 vcc, s14, v44\n"
      "s_nop 0\n"
      "s_or_b64 vcc, vcc, s[62:63]\n"
      "s_cbranch_vccz .Lk_gk\n"
      "s_waitcnt vmcnt(0) lgkmcnt(0)\n"
      "s_mov_b32 s68, 0x8000\n"
      ".Lk_gh:\n"
      "s_sleep 1\n"
      "buffer_load_dwordx4 v[10:13], v42, s[24:27], 0 offen offset:0 sc1\n"
      "buffer_load_dwordx4 v[14:17], v42, s[24:27], 0 offen offset:16 sc1\n"
      "buffer_load_dwordx4 v[18:21], v42, s[24:27], 0 offen offset:512 sc1\n"
      "buffer_load_dwordx4 v[22:25], v42, s[24:27], 0 offen offset:528 sc1\n"
      "buffer_load_dwordx4 v[26:29], v42, s[24:27], 0 offen offset:1024 sc1\n"
      "buffer_load_dwordx4 v[30:33], v42, s[24:27], 0 offen offset:1040 sc1\n"
      "buffer_load_dwordx4 v[34:37], v42, s[24:27], 0 offen offset:1536 sc1\n"
      "buffer_load_dwordx4 v[38:41], v42, s[24:27], 0 offen offset:1552 sc1\n"
      "s_waitcnt vmcnt(7)\n"
      "v_max_u32 v43, v11, v13\n"
      "v_min_u32 v44, v11, v13\n"
      "s_waitcnt vmcnt(6)\n"
      "v_max3_u32 v43, v43, v15, v17\n"
      "v_min3_u32 v44, v44, v15, v17\n"
      "s_waitcnt vmcnt(5)\n"
      "v_max3_u32 v43, v43, v19, v21\n"
      "v_min3_u32 v44, v44, v19, v21\n"
      "s_waitcnt vmcnt(4)\n"
      "v_max3_u32 v43, v43, v23, v25\n"
      "v_min3_u32 v44, v44, v23, v25\n"
      "s_waitcnt vmcnt(3)\n"
      "v_max3_u32 v43, v43, v27, v29\n"
      "v_min3_u32 v44, v44, v27, v29\n"
      "s_waitcnt vmcnt(2)\n"
      "v_max3_u32 v43, v43, v31, v33\n"
      "v_min3_u32 v44, v44, v31, v33\n"
      "s_waitcnt vmcnt(1)\n"
      "v_max3_u32 v43, v43, v35, v37\n"
      "v_min3_u32 v44, v44, v35, v37\n"
      "s_waitcnt vmcnt(0)\n"
      "v_max3_u32 v43, v43, v39, v41\n"
      "v_min3_u32 v44, v44, v39, v41\n"
      "v_cmp_ne_u32 s[62:63], s14, v43\n"
      "v_cmp_ne_u32 vcc, s14, v44\n"
      "s_nop 0\n"
      "s_or_b64 vcc, vcc, s[62:63]\n"
      "s_cbranch_vccz .Lk_gk\n"
      "s_sub_u32 s68, s68, 1\n"
      "s_cbranch_scc0 .Lk_gh\n"
      ".Lk_gk:\n"
      "v_mov_b32 v11, v12\n"
      "v_mov_b32 v12, v14\n"
      "v_mov_b32 v13, v16\n"
      "v_mov_b32 v14, v18\n"
      "v_mov_b32 v15, v20\n"
      "v_mov_b32 v16, v22\n"
      "v_mov_b32 v17, v24\n"
      "v_mov_b32 v18, v26\n"
      "v_mov_b32 v19, v28\n"
      "v_mov_b32 v20, v30\n"
      "v_mov_b32 v21, v32\n"
      "v_mov_b32 v22, v34\n"
      "v_mov_b32 v23, v36\n"
      "v_mov_b32 v24, v38\n"
      "v_mov_b32 v25, v40\n"
      "s_waitcnt vmcnt(10)\n"
      "s_add_u32 m0, s58, 0x3500\n"
      "s_nop 0\n"
      "buffer_load_dwordx4 v5, s[16:19], s59 offen offset:1792 nt lds\n"
      "s_mov_b32 exec_lo, 0xffffffff\n"
      "s_mov_b32 exec_hi, 0xf\n"
      "s_add_u32 m0, s93, 4608\n"
      "s_nop 0\n"
      "buffer_load_dwordx4 v235, s[44:47], 0 offen lds\n"
      "s_mov_b64 exec, -1\n"
      "v_mfma_f32_16x16x32_bf16 v[26:29], a[0:3], v[10:13], 0\n"
      "v_mfma_f32_16x16x32_bf16 v[26:29], a[4:7], v[14:17], v[26:29]\n"
      "v_mfma_f32_16x16x32_bf16 v[26:29], a[8:11], v[18:21], v[26:29]\n"
      "v_mfma_f32_16x16x32_bf16 v[26:29], a[12:15], v[22:25], v[26:29]\n"
      "v_mfma_f32_16x16x32_bf16 v[30:33], a[16:19], v[10:13], 0\n"
      "v_mfma_f32_16x16x32_bf16 v[30:33], a[20:23], v[14:17], v[30:33]\n"
      "v_mfma_f32_16x16x32_bf16 v[30:33], a[24:27], v[18:21], v[30:33]\n"
      "v_mfma_f32_16x16x32_bf16 v[30:33], a[28:31], v[22:25], v[30:33]\n"
      "v_and_b32 v1, 15, v0\n"
      "v_lshlrev_b32 v2, 4, v1\n"
      "v_bfe_u32 v1, v0, 4, 1\n"
      "v_lshlrev_b32 v1, 7, v1\n"
      "v_xor_b32 v2, v2, v1\n"
      "v_lshrrev_b32 v1, 4, v0\n"
      "v_lshl_add_u32 v2, v1, 11, v2\n"
      "v_xor_b32 v3, 0x20, v2\n"
      "v_xor_b32 v4, 0x40, v2\n"
      "v_xor_b32 v5, 0x60, v2\n"
      "s_waitcnt vmcnt(11)\n"
      "s_waitcnt lgkmcnt(8)\n"
      "s_lshl_b32 s59, s13, 13\n"
      "s_add_u32 m0, s58, 0x4000\n"
      "s_nop 0\n"
      "buffer_load_dwordx4 v2, s[20:23], s59 offen offset:0 nt lds\n"
      "v_mfma_f32_16x16x32_bf16 v[34:37], a[32:35], v[10:13], 0\n"
      "s_mov_b32 s66, 0x00030003\n"
      "s_mov_b32 s67, 0x00030003\n"
      "v_cndmask_b32 v38, v38, v26, s[66:67]\n"
      "v_mfma_f32_16x16x32_bf16 v[34:37], a[36:39], v[14:17], v[34:37]\n"
      "v_cndmask_b32 v39, v39, v27, s[66:67]\n"
      "v_mfma_f32_16x16x32_bf16 v[34:37], a[40:43], v[18:21], v[34:37]\n"
      "v_cndmask_b32 v40, v40, v28, s[66:67]\n"
      "v_mfma_f32_16x16x32_bf16 v[34:37], a[44:47], v[22:25], v[34:37]\n"
      "v_cndmask_b32 v41, v41, v29, s[66:67]\n"
      "v_mfma_f32_16x16x32_bf16 v[26:29], a[48:51], v[10:13], 0\n"
      "v_cndmask_b32 v42, v42, v30, s[66:67]\n"
      "v_mfma_f32_16x16x32_bf16 v[26:29], a[52:55], v[14:17], v[26:29]\n"
      "v_cndmask_b32 v43, v43, v31, s[66:67]\n"
      "v_mfma_f32_16x16x32_bf16 v[26:29], a[56:59], v[18:21], v[26:29]\n"
      "v_cndmask_b32 v44, v44, v32, s[66:67]\n"
      "v_mfma_f32_16x16x32_bf16 v[26:29], a[60:63], v[22:25], v[26:29]\n"
      "v_cndmask_b32 v45, v45, v33, s[66:67]\n"
      "s_waitcnt vmcnt(10)\n"
      "s_add_u32 m0, s58, 0x4300\n"
      "s_nop 0\n"
      "buffer_load_dwordx4 v3, s[20:23], s59 offen offset:256 nt lds\n"
      "v_mfma_f32_16x16x32_bf16 v[30:33], a[64:67], v[10:13], 0\n"
      "s_lshl_b64 s[66:67], s[66:67], 2\n"
      "v_cndmask_b32 v38, v38, v34, s[66:67]\n"
      "v_mfma_f32_16x16x32_bf16 v[30:33], a[68:71], v[14:17], v[30:33]\n"
      "v_cndmask_b32 v39, v39, v35, s[66:67]\n"
      "v_mfma_f32_16x16x32_bf16 v[30:33], a[72:75], v[18:21], v[30:33]\n"
      "v_cndmask_b32 v40, v40, v36, s[66:67]\n"
      "v_mfma_f32_16x16x32_bf16 v[30:33], a[76:79], v[22:25], v[30:33]\n"
      "v_cndmask_b32 v41, v41, v37, s[66:67]\n"
      "v_mfma_f32_16x16x32_bf16 v[34:37], a[80:83], v[10:13], 0\n"
      "v_cndmask_b32 v42, v42, v26, s[66:67]\n"
      "v_mfma_f32_16x16x32_bf16 v[34:37], a[84:87], v[14:17], v[34:37]\n"
      "v_cndmask_b32 v43, v43, v27, s[66:67]\n"
      "v_mfma_f32_16x16x32_bf16 v[34:37], a[88:91], v[18:21], v[34:37]\n"
      "v_cndmask_b32 v44, v44, v28, s[66:67]\n"
      "v_mfma_f32_16x16x32_bf16 v[34:37], a[92:95], v[22:25], v[34:37]\n"
      "v_cndmask_b32 v45, v45, v29, s[66:67]\n"
      "s_waitcnt vmcnt(9)\n"
      "s_add_u32 m0, s58, 0x4600\n"
      "s_nop 0\n"
      "buffer_load_dwordx4 v4, s[20:23], s59 offen offset:512 nt lds\n"
      "v_mfma_f32_16x16x32_bf16 v[26:29], a[96:99], v[10:13], 0\n"
      "s_lshl_b64 s[66:67], s[66:67], 2\n"
      "v_cndmask_b32 v38, v38, v30, s[66:67]\n"
      "v_mfma_f32_16x16x32_bf16 v[26:29], a[100:103], v[14:17], v[26:29]\n"
      "v_cndmask_b32 v39, v39, v31, s[66:67]\n"
      "v_mfma_f32_16x16x32_bf16 v[26:29], a[104:107], v[18:21], v[26:29]\n"
      "v_cndmask_b32 v40, v40, v32, s[66:67]\n"
      "v_mfma_f32_16x16x32_bf16 v[26:29], a[108:111], v[22:25], v[26:29]\n"
      "v_cndmask_b32 v41, v41, v33, s[66:67]\n"
      "v_mfma_f32_16x16x32_bf16 v[30:33], a[112:115], v[10:13], 0\n"
      "v_cndmask_b32 v42, v42, v34, s[66:67]\n"
      "v_mfma_f32_16x16x32_bf16 v[30:33], a[116:119], v[14:17], v[30:33]\n"
      "v_cndmask_b32 v43, v43, v35, s[66:67]\n"
      "v_mfma_f32_16x16x32_bf16 v[30:33], a[120:123], v[18:21], v[30:33]\n"
      "v_cndmask_b32 v44, v44, v36, s[66:67]\n"
      "v_mfma_f32_16x16x32_bf16 v[30:33], a[124:127], v[22:25], v[30:33]\n"
      "v_cndmask_b32 v45, v45, v37, s[66:67]\n"
      "s_waitcnt vmcnt(8)\n"
      "s_add_u32 m0, s58, 0x4900\n"
      "s_nop 0\n"
      "buffer_load_dwordx4 v5, s[20:23], s59 offen offset:768 nt lds\n"
      "v_mfma_f32_16x16x32_bf16 v[34:37], a[128:131], v[10:13], 0\n"
      "s_lshl_b64 s[66:67], s[66:67], 2\n"
      "v_cndmask_b32 v38, v38, v26, s[66:67]\n"
      "v_mfma_f32_16x16x32_bf16 v[34:37], a[132:135], v[14:17], v[34:37]\n"
      "v_cndmask_b32 v39, v39, v27, s[66:67]\n"
      "v_mfma_f32_16x16x32_bf16 v[34:37], a[136:139], v[18:21], v[34:37]\n"
      "v_cndmask_b32 v40, v40, v28, s[66:67]\n"
      "v_mfma_f32_16x16x32_bf16 v[34:37], a[140:143], v[22:25], v[34:37]\n"
      "v_cndmask_b32 v41, v41, v29, s[66:67]\n"
      "v_mfma_f32_16x16x32_bf16 v[26:29], a[144:147], v[10:13], 0\n"
      "v_cndmask_b32 v42, v42, v30, s[66:67]\n"
      "v_mfma_f32_16x16x32_bf16 v[26:29], a[148:151], v[14:17], v[26:29]\n"
      "v_cndmask_b32 v43, v43, v31, s[66:67]\n"
      "v_mfma_f32_16x16x32_bf16 v[26:29], a[152:155], v[18:21], v[26:29]\n"
      "v_cndmask_b32 v44, v44, v32, s[66:67]\n"
      "v_mfma_f32_16x16x32_bf16 v[26:29], a[156:159], v[22:25], v[26:29]\n"
      "v_cndmask_b32 v45, v45, v33, s[66:67]\n"
      "s_waitcnt vmcnt(7)\n"
      "s_add_u32 m0, s58, 0x4c00\n"
      "s_nop 0\n"
      "buffer_load_dwordx4 v2, s[20:23], s59 offen offset:1024 nt lds\n"
      "v_mfma_f32_16x16x32_bf16 v[30:33], a[160:163], v[10:13], 0\n"
      "s_lshl_b64 s[66:67], s[66:67], 2\n"
      "v_cndmask_b32 v38, v38, v34, s[66:67]\n"
      "v_mfma_f32_16x16x32_bf16 v[30:33], a[164:167], v[14:17], v[30:33]\n"
      "v_cndmask_b32 v39, v39, v35, s[66:67]\n"
      "v_mfma_f32_16x16x32_bf16 v[30:33], a[168:171], v[18:21], v[30:33]\n"
      "v_cndmask_b32 v40, v40, v36, s[66:67]\n"
      "v_mfma_f32_16x16x32_bf16 v[30:33], a[172:175], v[22:25], v[30:33]\n"
      "v_cndmask_b32 v41, v41, v37, s[66:67]\n"
      "v_mfma_f32_16x16x32_bf16 v[34:37], a[176:179], v[10:13], 0\n"
      "v_cndmask_b32 v42, v42, v26, s[66:67]\n"
      "v_mfma_f32_16x16x32_bf16 v[34:37], a[180:183], v[14:17], v[34:37]\n"
      "v_cndmask_b32 v43, v43, v27, s[66:67]\n"
      "v_mfma_f32_16x16x32_bf16 v[34:37], a[184:187], v[18:21], v[34:37]\n"
      "v_cndmask_b32 v44, v44, v28, s[66:67]\n"
      "v_mfma_f32_16x16x32_bf16 v[34:37], a[188:191], v[22:25], v[34:37]\n"
      "v_cndmask_b32 v45, v45, v29, s[66:67]\n"
      "s_waitcnt vmcnt(6)\n"
      "s_add_u32 m0, s58, 0x4f00\n"
      "s_nop 0\n"
      "buffer_load_dwordx4 v3, s[20:23], s59 offen offset:1280 nt lds\n"
      "ds_read_b128 a[224:227], v6 offset:8192\n"
      "ds_read_b128 a[228:231], v7 offset:8192\n"
      "ds_read_b128 a[232:235], v8 offset:8192\n"
      "ds_read_b128 a[236:239], v9 offset:8192\n"
      "ds_read_b128 a[240:243], v6 offset:12288\n"
      "ds_read_b128 a[244:247], v7 offset:12288\n"
      "ds_read_b128 a[248:251], v8 offset:12288\n"
      "ds_read_b128 a[252:255], v9 offset:12288\n"
      "s_waitcnt lgkmcnt(15)\n"
      "v_mfma_f32_16x16x32_bf16 v[26:29], a[192:195], v[10:13], 0\n"
      "s_lshl_b64 s[66:67], s[66:67], 2\n"
      "v_cndmask_b32 v38, v38, v30, s[66:67]\n"
      "s_waitcnt lgkmcnt(14)\n"
      "v_mfma_f32_16x16x32_bf16 v[26:29], a[196:199], v[14:17], v[26:29]\n"
      "v_cndmask_b32 v39, v39, v31, s[66:67]\n"
      "s_waitcnt lgkmcnt(13)\n"
      "v_mfma_f32_16x16x32_bf16 v[26:29], a[200:203], v[18:21], v[26:29]\n"
      "v_cndmask_b32 v40, v40, v32, s[66:67]\n"
      "s_waitcnt lgkmcnt(12)\n"
      "v_mfma_f32_16x16x32_bf16 v[26:29], a[204:207], v[22:25], v[26:29]\n"
      "v_cndmask_b32 v41, v41, v33, s[66:67]\n"
      "s_waitcnt lgkmcnt(11)\n"
      "v_mfma_f32_16x16x32_bf16 v[30:33], a[208:211], v[10:13], 0\n"
      "v_cndmask_b32 v42, v42, v34, s[66:67]\n"
      "s_waitcnt lgkmcnt(10)\n"
      "v_mfma_f32_16x16x32_bf16 v[30:33], a[212:215], v[14:17], v[30:33]\n"
      "v_cndmask_b32 v43, v43, v35, s[66:67]\n"
      "s_waitcnt lgkmcnt(9)\n"
      "v_mfma_f32_16x16x32_bf16 v[30:33], a[216:219], v[18:21], v[30:33]\n"
      "v_cndmask_b32 v44, v44, v36, s[66:67]\n"
      "s_waitcnt lgkmcnt(8)\n"
      "v_mfma_f32_16x16x32_bf16 v[30:33], a[220:223], v[22:25], v[30:33]\n"
      "v_cndmask_b32 v45, v45, v37, s[66:67]\n"
      "s_waitcnt vmcnt(5)\n"
      "s_add_u32 m0, s58, 0x5200\n"
      "s_nop 0\n"
      "buffer_load_dwordx4 v4, s[20:23], s59 offen offset:1536 nt lds\n"
      "s_waitcnt lgkmcnt(7)\n"
      "v_mfma_f32_16x16x32_bf16 v[34:37], a[224:227], v[10:13], 0\n"
      "s_lshl_b64 s[66:67], s[66:67], 2\n"
      "v_cndmask_b32 v38, v38, v26, s[66:67]\n"
      "s_waitcnt lgkmcnt(6)\n"
      "v_mfma_f32_16x16x32_bf16 v[34:37], a[228:231], v[14:17], v[34:37]\n"
      "v_cndmask_b32 v39, v39, v27, s[66:67]\n"
      "s_waitcnt lgkmcnt(5)\n"
      "v_mfma_f32_16x16x32_bf16 v[34:37], a[232:235], v[18:21], v[34:37]\n"
      "v_cndmask_b32 v40, v40, v28, s[66:67]\n"
      "s_waitcnt lgkmcnt(4)\n"
      "v_mfma_f32_16x16x32_bf16 v[34:37], a[236:239], v[22:25], v[34:37]\n"
      "v_cndmask_b32 v41, v41, v29, s[66:67]\n"
      "s_waitcnt lgkmcnt(3)\n"
      "v_mfma_f32_16x16x32_bf16 v[26:29], a[240:243], v[10:13], 0\n"
      "v_cndmask_b32 v42, v42, v30, s[66:67]\n"
      "s_waitcnt lgkmcnt(2)\n"
      "v_mfma_f32_16x16x32_bf16 v[26:29], a[244:247], v[14:17], v[26:29]\n"
      "v_cndmask_b32 v43, v43, v31, s[66:67]\n"
      "s_waitcnt lgkmcnt(1)\n"
      "v_mfma_f32_16x16x32_bf16 v[26:29], a[248:251], v[18:21], v[26:29]\n"
      "v_cndmask_b32 v44, v44, v32, s[66:67]\n"
      "s_waitcnt lgkmcnt(0)\n"
      "v_mfma_f32_16x16x32_bf16 v[26:29], a[252:255], v[22:25], v[26:29]\n"
      "v_cndmask_b32 v45, v45, v33, s[66:67]\n"
      "s_and_b32 s61, s13, 1\n"
      "s_lshl_b32 s61, s61, 5\n"
      "v_bfe_u32 v1, v0, 4, 1\n"
      "v_lshlrev_b32 v6, 7, v1\n"
      "v_bfe_u32 v1, v0, 2, 2\n"
      "v_lshl_add_u32 v6, v1, 5, v6\n"
      "v_bfe_u32 v1, v0, 1, 1\n"
      "v_lshl_add_u32 v6, v1, 4, v6\n"
      "v_and_b32 v1, 1, v0\n"
      "v_lshl_add_u32 v6, v1, 3, v6\n"
      "v_xor_b32 v6, s61, v6\n"
      "v_add_u32 v6, s58, v6\n"
      "v_bfe_u32 v1, v0, 2, 2\n"
      "v_lshl_add_u32 v6, v1, 10, v6\n"
      "v_lshrrev_b32 v1, 4, v0\n"
      "v_lshl_add_u32 v6, v1, 8, v6\n"
      "s_waitcnt vmcnt(5)\n"
      "s_add_u32 m0, s58, 0x5500\n"
      "s_nop 0\n"
      "buffer_load_dwordx4 v5, s[20:23], s59 offen offset:1792 nt lds\n"
      "s_waitcnt vmcnt(5)\n"
      "s_add_u32 s59, s59, 0x8000\n"
      "s_add_u32 m0, s58, 0x0\n"
      "s_nop 0\n"
      "buffer_load_dwordx4 v2, s[20:23], s59 offen offset:0 nt lds\n"
      "s_lshl_b64 s[66:67], s[66:67], 2\n"
      "v_cndmask_b32 v38, v38, v34, s[66:67]\n"
      "v_cndmask_b32 v39, v39, v35, s[66:67]\n"
      "v_cndmask_b32 v40, v40, v36, s[66:67]\n"
      "v_cndmask_b32 v41, v41, v37, s[66:67]\n"
      "v_cndmask_b32 v42, v42, v26, s[66:67]\n"
      "v_cndmask_b32 v43, v43, v27, s[66:67]\n"
      "v_cndmask_b32 v44, v44, v28, s[66:67]\n"
      "v_cndmask_b32 v45, v45, v29, s[66:67]\n"
      "v_max3_f32 v46, v38, v39, v40\n"
      "v_max3_f32 v26, v41, v42, v43\n"
      "v_max3_f32 v46, v46, v44, v45\n"
      "v_max_f32 v46, v46, v26\n"
      "v_mov_b32 v10, 0\n"
      "v_mov_b32 v11, 0\n"
      "v_max_f32_dpp v46, v46, v46 row_ror:2 row_mask:0xf bank_mask:0xf\n"
      "v_mov_b32 v12, 0\n"
      "v_mov_b32 v13, 0\n"
      "v_max_f32_dpp v46, v46, v46 row_ror:4 row_mask:0xf bank_mask:0xf\n"
      "v_mov_b32 v14, 0\n"
      "v_mov_b32 v15, 0\n"
      "v_max_f32_dpp v46, v46, v46 row_ror:8 row_mask:0xf bank_mask:0xf\n"
      "v_mov_b32 v26, v46\n"
      "v_mov_b32 v16, 0\n"
      "v_mov_b32 v17, 0\n"
      "v_permlane16_swap_b32 v46, v26\n"
      "v_max_f32 v46, v46, v26\n"
      "v_mov_b32 v26, v46\n"
      "v_mov_b32 v18, 0\n"
      "v_mov_b32 v19, 0\n"
      "v_permlane32_swap_b32 v46, v26\n"
      "v_max_f32 v46, v46, v26\n"
      "v_mul_f32 v46, s71, v46\n"
      "v_fma_f32 v38, v38, s71, -v46\n"
      "v_fma_f32 v39, v39, s71, -v46\n"
      "v_fma_f32 v40, v40, s71, -v46\n"
      "v_fma_f32 v41, v41, s71, -v46\n"
      "v_fma_f32 v42, v42, s71, -v46\n"
      "v_fma_f32 v43, v43, s71, -v46\n"
      "v_fma_f32 v44, v44, s71, -v46\n"
      "v_fma_f32 v45, v45, s71, -v46\n"
      "v_exp_f32 v38, v38\n"
      "v_exp_f32 v39, v39\n"
      "v_exp_f32 v40, v40\n"
      "v_exp_f32 v41, v41\n"
      "v_exp_f32 v42, v42\n"
      "v_exp_f32 v43, v43\n"
      "v_exp_f32 v44, v44\n"
      "v_exp_f32 v45, v45\n"
      "v_pk_add_f32 v[26:27], v[38:39], v[40:41]\n"
      "v_pk_add_f32 v[28:29], v[42:43], v[44:45]\n"
      "v_cvt_pk_bf16_f32 v38, v38, v39\n"
      "v_cvt_pk_bf16_f32 v39, v40, v41\n"
      "v_cvt_pk_bf16_f32 v40, v42, v43\n"
      "v_cvt_pk_bf16_f32 v41, v44, v45\n"
      "v_pk_add_f32 v[26:27], v[26:27], v[28:29]\n"
      "v_add_f32 v47, v26, v27\n"
      "v_mov_b32 v20, 0\n"
      "v_mov_b32 v21, 0\n"
      "v_mov_b32 v22, 0\n"
      "v_mov_b32 v23, 0\n"
      "v_mov_b32 v24, 0\n"
      "v_mov_b32 v25, 0\n"
      "s_waitcnt vmcnt(5)\n"
      "s_add_u32 m0, s58, 0x300\n"
      "s_nop 0\n"
      "buffer_load_dwordx4 v3, s[20:23], s59 offen offset:256 nt lds\n"
      "v_add_f32_dpp v47, v47, v47 row_ror:2 row_mask:0xf bank_mask:0xf\n"
      "s_nop 1\n"
      "v_add_f32_dpp v47, v47, v47 row_ror:4 row_mask:0xf bank_mask:0xf\n"
      "s_nop 1\n"
      "v_add_f32_dpp v47, v47, v47 row_ror:8 row_mask:0xf bank_mask:0xf\n"
      "v_mov_b32 v42, v47\n"
      "s_nop 1\n"
      "v_permlane16_swap_b32 v47, v42\n"
      "v_add_f32 v47, v47, v42\n"
      "v_mov_b32 v42, v47\n"
      "s_nop 1\n"
      "v_permlane32_swap_b32 v47, v42\n"
      "v_add_f32 v47, v47, v42\n"
      "s_waitcnt vmcnt(5)\n"
      "s_add_u32 m0, s58, 0x600\n"
      "s_nop 0\n"
      "buffer_load_dwordx4 v4, s[20:23], s59 offen offset:512 nt lds\n"
      "s_waitcnt vmcnt(5)\n"
      "s_add_u32 m0, s58, 0x900\n"
      "s_nop 0\n"
      "buffer_load_dwordx4 v5, s[20:23], s59 offen offset:768 nt lds\n"
      "s_waitcnt vmcnt(5)\n"
      "s_add_u32 m0, s58, 0xc00\n"
      "s_nop 0\n"
      "buffer_load_dwordx4 v2, s[20:23], s59 offen offset:1024 nt lds\n"
      "s_waitcnt vmcnt(5)\n"
      "s_add_u32 m0, s58, 0xf00\n"
      "s_nop 0\n"
      "buffer_load_dwordx4 v3, s[20:23], s59 offen offset:1280 nt lds\n"
      "ds_read_b64_tr_b16 v[96:97], v6 offset:16384\n"
      "ds_read_b64_tr_b16 v[98:99], v6 offset:20480\n"
      "v_xor_b32 v8, 0x20, v6\n"
      "ds_read_b64_tr_b16 v[100:101], v8 offset:16384\n"
      "ds_read_b64_tr_b16 v[102:103], v8 offset:20480\n"
      "v_xor_b32 v7, 0x40, v6\n"
      "ds_read_b64_tr_b16 v[104:105], v7 offset:16384\n"
      "ds_read_b64_tr_b16 v[106:107], v7 offset:20480\n"
      "v_xor_b32 v8, 0x60, v6\n"
      "ds_read_b64_tr_b16 v[108:109], v8 offset:16384\n"
      "ds_read_b64_tr_b16 v[110:111], v8 offset:20480\n"
      "v_xor_b32 v7, 0x80, v6\n"
      "ds_read_b64_tr_b16 v[112:113], v7 offset:16384\n"
      "ds_read_b64_tr_b16 v[114:115], v7 offset:20480\n"
      "v_xor_b32 v8, 0xa0, v6\n"
      "ds_read_b64_tr_b16 v[116:117], v8 offset:16384\n"
      "ds_read_b64_tr_b16 v[118:119], v8 offset:20480\n"
      "v_xor_b32 v7, 0xc0, v6\n"
      "ds_read_b64_tr_b16 v[120:121], v7 offset:16384\n"
      "ds_read_b64_tr_b16 v[122:123], v7 offset:20480\n"
      "v_xor_b32 v8, 0xe0, v6\n"
      "ds_read_b64_tr_b16 v[124:125], v8 offset:16384\n"
      "ds_read_b64_tr_b16 v[126:127], v8 offset:20480\n"
      "s_waitcnt vmcnt(5)\n"
      "s_add_u32 m0, s58, 0x1200\n"
      "s_nop 0\n"
      "buffer_load_dwordx4 v4, s[20:23], s59 offen offset:1536 nt lds\n"
      "s_waitcnt vmcnt(5)\n"
      "s_add_u32 m0, s58, 0x1500\n"
      "s_nop 0\n"
      "buffer_load_dwordx4 v5, s[20:23], s59 offen offset:1792 nt lds\n"
      "v_mov_b32_dpp v10, v38 quad_perm:[0,1,2,3] row_mask:0xf bank_mask:0x1\n"
      "v_mov_b32_dpp v11, v39 quad_perm:[0,1,2,3] row_mask:0xf bank_mask:0x1\n"
      "v_mov_b32_dpp v12, v40 quad_perm:[0,1,2,3] row_mask:0xf bank_mask:0x1\n"
      "v_mov_b32_dpp v13, v41 quad_perm:[0,1,2,3] row_mask:0xf bank_mask:0x1\n"
      "s_waitcnt lgkmcnt(14)\n"
      "s_nop 0\n"
      "v_mfma_f32_16x16x32_bf16 v[48:51], v[10:13], v[96:99], 0\n"
      "v_mov_b32_dpp v14, v38 row_shr:4 row_mask:0xf bank_mask:0x2\n"
      "v_mov_b32_dpp v15, v39 row_shr:4 row_mask:0xf bank_mask:0x2\n"
      "v_mov_b32_dpp v16, v40 row_shr:4 row_mask:0xf bank_mask:0x2\n"
      "v_mov_b32_dpp v17, v41 row_shr:4 row_mask:0xf bank_mask:0x2\n"
      "s_waitcnt lgkmcnt(12)\n"
      "v_mfma_f32_16x16x32_bf16 v[52:55], v[10:13], v[100:103], 0\n"
      "s_waitcnt lgkmcnt(10)\n"
      "v_mfma_f32_16x16x32_bf16 v[48:51], v[14:17], v[104:107], v[48:51]\n"
      "v_mov_b32_dpp v18, v38 row_shr:8 row_mask:0xf bank_mask:0x4\n"
      "v_mov_b32_dpp v19, v39 row_shr:8 row_mask:0xf bank_mask:0x4\n"
      "v_mov_b32_dpp v20, v40 row_shr:8 row_mask:0xf bank_mask:0x4\n"
      "v_mov_b32_dpp v21, v41 row_shr:8 row_mask:0xf bank_mask:0x4\n"
      "s_waitcnt lgkmcnt(8)\n"
      "v_mfma_f32_16x16x32_bf16 v[52:55], v[14:17], v[108:111], v[52:55]\n"
      "s_waitcnt lgkmcnt(6)\n"
      "v_mfma_f32_16x16x32_bf16 v[48:51], v[18:21], v[112:115], v[48:51]\n"
      "v_mov_b32_dpp v22, v38 row_shr:12 row_mask:0xf bank_mask:0x8\n"
      "v_mov_b32_dpp v23, v39 row_shr:12 row_mask:0xf bank_mask:0x8\n"
      "v_mov_b32_dpp v24, v40 row_shr:12 row_mask:0xf bank_mask:0x8\n"
      "v_mov_b32_dpp v25, v41 row_shr:12 row_mask:0xf bank_mask:0x8\n"
      "s_waitcnt lgkmcnt(4)\n"
      "v_mfma_f32_16x16x32_bf16 v[52:55], v[18:21], v[116:119], v[52:55]\n"
      "s_waitcnt lgkmcnt(2)\n"
      "v_mfma_f32_16x16x32_bf16 v[48:51], v[22:25], v[120:123], v[48:51]\n"
      "s_waitcnt lgkmcnt(0)\n"
      "v_mfma_f32_16x16x32_bf16 v[52:55], v[22:25], v[124:127], v[52:55]\n"
      "s_waitcnt vmcnt(5)\n"
      "s_add_u32 s59, s59, 0x8000\n"
      "s_add_u32 m0, s58, 0x2000\n"
      "s_nop 0\n"
      "buffer_load_dwordx4 v2, s[20:23], s59 offen offset:0 nt lds\n"
      "s_waitcnt vmcnt(5)\n"
      "s_add_u32 m0, s58, 0x2300\n"
      "s_nop 0\n"
      "buffer_load_dwordx4 v3, s[20:23], s59 offen offset:256 nt lds\n"
      "s_waitcnt vmcnt(5)\n"
      "s_add_u32 m0, s58, 0x2600\n"
      "s_nop 0\n"
      "buffer_load_dwordx4 v4, s[20:23], s59 offen offset:512 nt lds\n"
      "s_waitcnt vmcnt(5)\n"
      "s_add_u32 m0, s58, 0x2900\n"
      "s_nop 0\n"
      "buffer_load_dwordx4 v5, s[20:23], s59 offen offset:768 nt lds\n"
      "s_waitcnt vmcnt(5)\n"
      "s_add_u32 m0, s58, 0x2c00\n"
      "s_nop 0\n"
      "buffer_load_dwordx4 v2, s[20:23], s59 offen offset:1024 nt lds\n"
      "s_waitcnt vmcnt(5)\n"
      "s_add_u32 m0, s58, 0x2f00\n"
      "s_nop 0\n"
      "buffer_load_dwordx4 v3, s[20:23], s59 offen offset:1280 nt lds\n"
      "ds_read_b64_tr_b16 v[128:129], v6 offset:0\n"
      "ds_read_b64_tr_b16 v[130:131], v6 offset:4096\n"
      "v_xor_b32 v8, 0x20, v6\n"
      "ds_read_b64_tr_b16 v[132:133], v8 offset:0\n"
      "ds_read_b64_tr_b16 v[134:135], v8 offset:4096\n"
      "v_xor_b32 v7, 0x40, v6\n"
      "ds_read_b64_tr_b16 v[136:137], v7 offset:0\n"
      "ds_read_b64_tr_b16 v[138:139], v7 offset:4096\n"
      "v_xor_b32 v8, 0x60, v6\n"
      "ds_read_b64_tr_b16 v[140:141], v8 offset:0\n"
      "ds_read_b64_tr_b16 v[142:143], v8 offset:4096\n"
      "v_xor_b32 v7, 0x80, v6\n"
      "ds_read_b64_tr_b16 v[144:145], v7 offset:0\n"
      "ds_read_b64_tr_b16 v[146:147], v7 offset:4096\n"
      "v_xor_b32 v8, 0xa0, v6\n"
      "ds_read_b64_tr_b16 v[148:149], v8 offset:0\n"
      "ds_read_b64_tr_b16 v[150:151], v8 offset:4096\n"
      "v_xor_b32 v7, 0xc0, v6\n"
      "ds_read_b64_tr_b16 v[152:153], v7 offset:0\n"
      "ds_read_b64_tr_b16 v[154:155], v7 offset:4096\n"
      "v_xor_b32 v8, 0xe0, v6\n"
      "ds_read_b64_tr_b16 v[156:157], v8 offset:0\n"
      "ds_read_b64_tr_b16 v[158:159], v8 offset:4096\n"
      "s_waitcnt vmcnt(5)\n"
      "s_add_u32 m0, s58, 0x3200\n"
      "s_nop 0\n"
      "buffer_load_dwordx4 v4, s[20:23], s59 offen offset:1536 nt lds\n"
      "s_waitcnt vmcnt(5)\n"
      "s_add_u32 m0, s58, 0x3500\n"
      "s_nop 0\n"
      "buffer_load_dwordx4 v5, s[20:23], s59 offen offset:1792 nt lds\n"
      "v_mov_b32_dpp v10, v38 row_shl:2 row_mask:0xf bank_mask:0x1\n"
      "v_mov_b32_dpp v11, v39 row_shl:2 row_mask:0xf bank_mask:0x1\n"
      "v_mov_b32_dpp v12, v40 row_shl:2 row_mask:0xf bank_mask:0x1\n"
      "v_mov_b32_dpp v13, v41 row_shl:2 row_mask:0xf bank_mask:0x1\n"
      "s_waitcnt lgkmcnt(14)\n"
      "s_nop 0\n"
      "v_mfma_f32_16x16x32_bf16 v[48:51], v[10:13], v[128:131], v[48:51]\n"
      "v_mov_b32_dpp v14, v38 row_shr:2 row_mask:0xf bank_mask:0x2\n"
      "v_mov_b32_dpp v15, v39 row_shr:2 row_mask:0xf bank_mask:0x2\n"
      "v_mov_b32_dpp v16, v40 row_shr:2 row_mask:0xf bank_mask:0x2\n"
      "v_mov_b32_dpp v17, v41 row_shr:2 row_mask:0xf bank_mask:0x2\n"
      "s_waitcnt lgkmcnt(12)\n"
      "v_mfma_f32_16x16x32_bf16 v[52:55], v[10:13], v[132:135], v[52:55]\n"
      "s_waitcnt lgkmcnt(10)\n"
      "v_mfma_f32_16x16x32_bf16 v[48:51], v[14:17], v[136:139], v[48:51]\n"
      "v_mov_b32_dpp v18, v38 row_shr:6 row_mask:0xf bank_mask:0x4\n"
      "v_mov_b32_dpp v19, v39 row_shr:6 row_mask:0xf bank_mask:0x4\n"
      "v_mov_b32_dpp v20, v40 row_shr:6 row_mask:0xf bank_mask:0x4\n"
      "v_mov_b32_dpp v21, v41 row_shr:6 row_mask:0xf bank_mask:0x4\n"
      "s_waitcnt lgkmcnt(8)\n"
      "v_mfma_f32_16x16x32_bf16 v[52:55], v[14:17], v[140:143], v[52:55]\n"
      "s_waitcnt lgkmcnt(6)\n"
      "v_mfma_f32_16x16x32_bf16 v[48:51], v[18:21], v[144:147], v[48:51]\n"
      "v_mov_b32_dpp v22, v38 row_shr:10 row_mask:0xf bank_mask:0x8\n"
      "v_mov_b32_dpp v23, v39 row_shr:10 row_mask:0xf bank_mask:0x8\n"
      "v_mov_b32_dpp v24, v40 row_shr:10 row_mask:0xf bank_mask:0x8\n"
      "v_mov_b32_dpp v25, v41 row_shr:10 row_mask:0xf bank_mask:0x8\n"
      "s_waitcnt lgkmcnt(4)\n"
      "v_mfma_f32_16x16x32_bf16 v[52:55], v[18:21], v[148:151], v[52:55]\n"
      "s_waitcnt lgkmcnt(2)\n"
      "v_mfma_f32_16x16x32_bf16 v[48:51], v[22:25], v[152:155], v[48:51]\n"
      "s_waitcnt lgkmcnt(0)\n"
      "v_mfma_f32_16x16x32_bf16 v[52:55], v[22:25], v[156:159], v[52:55]\n"
      "s_waitcnt vmcnt(5)\n"
      "s_add_u32 s59, s59, 0x8000\n"
      "s_add_u32 m0, s58, 0x4000\n"
      "s_nop 0\n"
      "buffer_load_dwordx4 v2, s[20:23], s59 offen offset:0 nt lds\n"
      "s_waitcnt vmcnt(5)\n"
      "s_add_u32 m0, s58, 0x4300\n"
      "s_nop 0\n"
      "buffer_load_dwordx4 v3, s[20:23], s59 offen offset:256 nt lds\n"
      "s_waitcnt vmcnt(5)\n"
      "s_add_u32 m0, s58, 0x4600\n"
      "s_nop 0\n"
      "buffer_load_dwordx4 v4, s[20:23], s59 offen offset:512 nt lds\n"
      "s_waitcnt vmcnt(5)\n"
      "s_add_u32 m0, s58, 0x4900\n"
      "s_nop 0\n"
      "buffer_load_dwordx4 v5, s[20:23], s59 offen offset:768 nt lds\n"
      "s_waitcnt vmcnt(5)\n"
      "s_add_u32 m0, s58, 0x4c00\n"
      "s_nop 0\n"
      "buffer_load_dwordx4 v2, s[20:23], s59 offen offset:1024 nt lds\n"
      "s_waitcnt vmcnt(5)\n"
      "s_add_u32 m0, s58, 0x4f00\n"
      "s_nop 0\n"
      "buffer_load_dwordx4 v3, s[20:23], s59 offen offset:1280 nt lds\n"
      "ds_read_b64_tr_b16 v[160:161], v6 offset:8192\n"
      "ds_read_b64_tr_b16 v[162:163], v6 offset:12288\n"
      "v_xor_b32 v8, 0x20, v6\n"
      "ds_read_b64_tr_b16 v[164:165], v8 offset:8192\n"
      "ds_read_b64_tr_b16 v[166:167], v8 offset:12288\n"
      "v_xor_b32 v7, 0x40, v6\n"
      "ds_read_b64_tr_b16 v[168:169], v7 offset:8192\n"
      "ds_read_b64_tr_b16 v[170:171], v7 offset:12288\n"
      "v_xor_b32 v8, 0x60, v6\n"
      "ds_read_b64_tr_b16 v[172:173], v8 offset:8192\n"
      "ds_read_b64_tr_b16 v[174:175], v8 offset:12288\n"
      "v_xor_b32 v7, 0x80, v6\n"
      "ds_read_b64_tr_b16 v[176:177], v7 offset:8192\n"
      "ds_read_b64_tr_b16 v[178:179], v7 offset:12288\n"
      "v_xor_b32 v8, 0xa0, v6\n"
      "ds_read_b64_tr_b16 v[180:181], v8 offset:8192\n"
      "ds_read_b64_tr_b16 v[182:183], v8 offset:12288\n"
      "v_xor_b32 v7, 0xc0, v6\n"
      "ds_read_b64_tr_b16 v[184:185], v7 offset:8192\n"
      "ds_read_b64_tr_b16 v[186:187], v7 offset:12288\n"
      "v_xor_b32 v8, 0xe0, v6\n"
      "ds_read_b64_tr_b16 v[188:189], v8 offset:8192\n"
      "ds_read_b64_tr_b16 v[190:191], v8 offset:12288\n"
      "s_waitcnt vmcnt(5)\n"
      "s_add_u32 m0, s58, 0x5200\n"
      "s_nop 0\n"
      "buffer_load_dwordx4 v4, s[20:23], s59 offen offset:1536 nt lds\n"
      "s_waitcnt vmcnt(5)\n"
      "s_add_u32 m0, s58, 0x5500\n"
      "s_nop 0\n"
      "buffer_load_dwordx4 v5, s[20:23], s59 offen offset:1792 nt lds\n"
      "v_mov_b32_dpp v10, v38 row_shl:4 row_mask:0xf bank_mask:0x1\n"
      "v_mov_b32_dpp v11, v39 row_shl:4 row_mask:0xf bank_mask:0x1\n"
      "v_mov_b32_dpp v12, v40 row_shl:4 row_mask:0xf bank_mask:0x1\n"
      "v_mov_b32_dpp v13, v41 row_shl:4 row_mask:0xf bank_mask:0x1\n"
      "s_waitcnt lgkmcnt(14)\n"
      "s_nop 0\n"
      "v_mfma_f32_16x16x32_bf16 v[48:51], v[10:13], v[160:163], v[48:51]\n"
      "v_mov_b32_dpp v14, v38 quad_perm:[0,1,2,3] row_mask:0xf bank_mask:0x2\n"
      "v_mov_b32_dpp v15, v39 quad_perm:[0,1,2,3] row_mask:0xf bank_mask:0x2\n"
      "v_mov_b32_dpp v16, v40 quad_perm:[0,1,2,3] row_mask:0xf bank_mask:0x2\n"
      "v_mov_b32_dpp v17, v41 quad_perm:[0,1,2,3] row_mask:0xf bank_mask:0x2\n"
      "s_waitcnt lgkmcnt(12)\n"
      "v_mfma_f32_16x16x32_bf16 v[52:55], v[10:13], v[164:167], v[52:55]\n"
      "s_waitcnt lgkmcnt(10)\n"
      "v_mfma_f32_16x16x32_bf16 v[48:51], v[14:17], v[168:171], v[48:51]\n"
      "v_mov_b32_dpp v18, v38 row_shr:4 row_mask:0xf bank_mask:0x4\n"
      "v_mov_b32_dpp v19, v39 row_shr:4 row_mask:0xf bank_mask:0x4\n"
      "v_mov_b32_dpp v20, v40 row_shr:4 row_mask:0xf bank_mask:0x4\n"
      "v_mov_b32_dpp v21, v41 row_shr:4 row_mask:0xf bank_mask:0x4\n"
      "s_waitcnt lgkmcnt(8)\n"
      "v_mfma_f32_16x16x32_bf16 v[52:55], v[14:17], v[172:175], v[52:55]\n"
      "s_waitcnt lgkmcnt(6)\n"
      "v_mfma_f32_16x16x32_bf16 v[48:51], v[18:21], v[176:179], v[48:51]\n"
      "v_mov_b32_dpp v22, v38 row_shr:8 row_mask:0xf bank_mask:0x8\n"
      "v_mov_b32_dpp v23, v39 row_shr:8 row_mask:0xf bank_mask:0x8\n"
      "v_mov_b32_dpp v24, v40 row_shr:8 row_mask:0xf bank_mask:0x8\n"
      "v_mov_b32_dpp v25, v41 row_shr:8 row_mask:0xf bank_mask:0x8\n"
      "s_waitcnt lgkmcnt(4)\n"
      "v_mfma_f32_16x16x32_bf16 v[52:55], v[18:21], v[180:183], v[52:55]\n"
      "s_waitcnt lgkmcnt(2)\n"
      "v_mfma_f32_16x16x32_bf16 v[48:51], v[22:25], v[184:187], v[48:51]\n"
      "s_waitcnt lgkmcnt(0)\n"
      "v_mfma_f32_16x16x32_bf16 v[52:55], v[22:25], v[188:191], v[52:55]\n"
      "s_waitcnt vmcnt(5)\n"
      "s_add_u32 s59, s59, 0x8000\n"
      "s_add_u32 m0, s58, 0x0\n"
      "s_nop 0\n"
      "buffer_load_dwordx4 v2, s[20:23], s59 offen offset:0 nt lds\n"
      "s_cmp_lg_u32 s13, 0\n"
      "s_cbranch_scc1 .Lk_fa\n"
      "s_mov_b32 s60, 0x1000\n"
      "v_lshl_add_u32 v36, v0, 4, s60\n"
      "buffer_load_dwordx4 v[26:29], v36, s[24:27], 0 offen offset:0 sc1\n"
      "buffer_load_dwordx4 v[30:33], v36, s[24:27], 0 offen offset:1024 sc1\n"
      ".Lk_fa:\n"
      "s_waitcnt vmcnt(5)\n"
      "s_add_u32 m0, s58, 0x300\n"
      "s_nop 0\n"
      "buffer_load_dwordx4 v3, s[20:23], s59 offen offset:256 nt lds\n"
      "s_waitcnt vmcnt(5)\n"
      "s_add_u32 m0, s58, 0x600\n"
      "s_nop 0\n"
      "buffer_load_dwordx4 v4, s[20:23], s59 offen offset:512 nt lds\n"
      "s_waitcnt vmcnt(5)\n"
      "s_add_u32 m0, s58, 0x900\n"
      "s_nop 0\n"
      "buffer_load_dwordx4 v5, s[20:23], s59 offen offset:768 nt lds\n"
      "s_waitcnt vmcnt(5)\n"
      "s_add_u32 m0, s58, 0xc00\n"
      "s_nop 0\n"
      "buffer_load_dwordx4 v2, s[20:23], s59 offen offset:1024 nt lds\n"
      "s_waitcnt vmcnt(5)\n"
      "s_add_u32 m0, s58, 0xf00\n"
      "s_nop 0\n"
      "buffer_load_dwordx4 v3, s[20:23], s59 offen offset:1280 nt lds\n"
      "ds_read_b64_tr_b16 v[192:193], v6 offset:16384\n"
      "ds_read_b64_tr_b16 v[194:195], v6 offset:20480\n"
      "v_xor_b32 v8, 0x20, v6\n"
      "ds_read_b64_tr_b16 v[196:197], v8 offset:16384\n"
      "ds_read_b64_tr_b16 v[198:199], v8 offset:20480\n"
      "v_xor_b32 v7, 0x40, v6\n"
      "ds_read_b64_tr_b16 v[200:201], v7 offset:16384\n"
      "ds_read_b64_tr_b16 v[202:203], v7 offset:20480\n"
      "v_xor_b32 v8, 0x60, v6\n"
      "ds_read_b64_tr_b16 v[204:205], v8 offset:16384\n"
      "ds_read_b64_tr_b16 v[206:207], v8 offset:20480\n"
      "v_xor_b32 v7, 0x80, v6\n"
      "ds_read_b64_tr_b16 v[208:209], v7 offset:16384\n"
      "ds_read_b64_tr_b16 v[210:211], v7 offset:20480\n"
      "v_xor_b32 v8, 0xa0, v6\n"
      "ds_read_b64_tr_b16 v[212:213], v8 offset:16384\n"
      "ds_read_b64_tr_b16 v[214:215], v8 offset:20480\n"
      "v_xor_b32 v7, 0xc0, v6\n"
      "ds_read_b64_tr_b16 v[216:217], v7 offset:16384\n"
      "ds_read_b64_tr_b16 v[218:219], v7 offset:20480\n"
      "v_xor_b32 v8, 0xe0, v6\n"
      "ds_read_b64_tr_b16 v[220:221], v8 offset:16384\n"
      "ds_read_b64_tr_b16 v[222:223], v8 offset:20480\n"
      "s_waitcnt vmcnt(5)\n"
      "s_add_u32 m0, s58, 0x1200\n"
      "s_nop 0\n"
      "buffer_load_dwordx4 v4, s[20:23], s59 offen offset:1536 nt lds\n"
      "s_cmp_lg_u32 s13, 0\n"
      "s_cbranch_scc1 .Lk_fb\n"
      "v_max3_u32 v34, v26, v27, v28\n"
      "v_min3_u32 v35, v26, v27, v28\n"
      "v_max3_u32 v34, v34, v29, v30\n"
      "v_min3_u32 v35, v35, v29, v30\n"
      "v_max3_u32 v34, v34, v31, v32\n"
      "v_min3_u32 v35, v35, v31, v32\n"
      "v_max_u32 v34, v34, v33\n"
      "v_min_u32 v35, v35, v33\n"
      "v_cmp_ne_u32 s[62:63], s14, v34\n"
      "v_cmp_ne_u32 vcc, s14, v35\n"
      "s_nop 0\n"
      "s_or_b64 vcc, vcc, s[62:63]\n"
      "s_cbranch_vccz .Lk_fk\n"
      "s_waitcnt vmcnt(0) lgkmcnt(0)\n"
      "s_mov_b32 s68, 0x8000\n"
      ".Lk_fh:\n"
      "s_sleep 1\n"
      "buffer_load_dwordx4 v[26:29], v36, s[24:27], 0 offen offset:0 sc1\n"
      "buffer_load_dwordx4 v[30:33], v36, s[24:27], 0 offen offset:1024 sc1\n"
      "s_waitcnt vmcnt(1)\n"
      "v_max3_u32 v34, v26, v27, v28\n"
      "v_min3_u32 v35, v26, v27, v28\n"
      "s_waitcnt vmcnt(0)\n"
      "v_max3_u32 v34, v34, v29, v30\n"
      "v_min3_u32 v35, v35, v29, v30\n"
      "v_max3_u32 v34, v34, v31, v32\n"
      "v_min3_u32 v35, v35, v31, v32\n"
      "v_max_u32 v34, v34, v33\n"
      "v_min_u32 v35, v35, v33\n"
      "v_cmp_ne_u32 s[62:63], s14, v34\n"
      "v_cmp_ne_u32 vcc, s14, v35\n"
      "s_nop 0\n"
      "s_or_b64 vcc, vcc, s[62:63]\n"
      "s_cbranch_vccz .Lk_fk\n"
      "s_sub_u32 s68, s68, 1\n"
      "s_cbranch_scc0 .Lk_fh\n"
      ".Lk_fk:\n"
      ".Lk_fb:\n"
      "s_waitcnt vmcnt(5)\n"
      "s_add_u32 m0, s58, 0x1500\n"
      "s_nop 0\n"
      "buffer_load_dwordx4 v5, s[20:23], s59 offen offset:1792 nt lds\n"
      "v_mov_b32_dpp v10, v38 row_shl:6 row_mask:0xf bank_mask:0x1\n"
      "v_mov_b32_dpp v11, v39 row_shl:6 row_mask:0xf bank_mask:0x1\n"
      "v_mov_b32_dpp v12, v40 row_shl:6 row_mask:0xf bank_mask:0x1\n"
      "v_mov_b32_dpp v13, v41 row_shl:6 row_mask:0xf bank_mask:0x1\n"
      "s_waitcnt lgkmcnt(14)\n"
      "s_nop 0\n"
      "v_mfma_f32_16x16x32_bf16 v[48:51], v[10:13], v[192:195], v[48:51]\n"
      "v_mov_b32_dpp v14, v38 row_shl:2 row_mask:0xf bank_mask:0x2\n"
      "v_mov_b32_dpp v15, v39 row_shl:2 row_mask:0xf bank_mask:0x2\n"
      "v_mov_b32_dpp v16, v40 row_shl:2 row_mask:0xf bank_mask:0x2\n"
      "v_mov_b32_dpp v17, v41 row_shl:2 row_mask:0xf bank_mask:0x2\n"
      "s_waitcnt lgkmcnt(12)\n"
      "v_mfma_f32_16x16x32_bf16 v[52:55], v[10:13], v[196:199], v[52:55]\n"
      "s_waitcnt lgkmcnt(10)\n"
      "v_mfma_f32_16x16x32_bf16 v[48:51], v[14:17], v[200:203], v[48:51]\n"
      "v_mov_b32_dpp v18, v38 row_shr:2 row_mask:0xf bank_mask:0x4\n"
      "v_mov_b32_dpp v19, v39 row_shr:2 row_mask:0xf bank_mask:0x4\n"
      "v_mov_b32_dpp v20, v40 row_shr:2 row_mask:0xf bank_mask:0x4\n"
      "v_mov_b32_dpp v21, v41 row_shr:2 row_mask:0xf bank_mask:0x4\n"
      "s_waitcnt lgkmcnt(8)\n"
      "v_mfma_f32_16x16x32_bf16 v[52:55], v[14:17], v[204:207], v[52:55]\n"
      "s_waitcnt lgkmcnt(6)\n"
      "v_mfma_f32_16x16x32_bf16 v[48:51], v[18:21], v[208:211], v[48:51]\n"
      "v_mov_b32_dpp v22, v38 row_shr:6 row_mask:0xf bank_mask:0x8\n"
      "v_mov_b32_dpp v23, v39 row_shr:6 row_mask:0xf bank_mask:0x8\n"
      "v_mov_b32_dpp v24, v40 row_shr:6 row_mask:0xf bank_mask:0x8\n"
      "v_mov_b32_dpp v25, v41 row_shr:6 row_mask:0xf bank_mask:0x8\n"
      "s_waitcnt lgkmcnt(4)\n"
      "v_mfma_f32_16x16x32_bf16 v[52:55], v[18:21], v[212:215], v[52:55]\n"
      "s_waitcnt lgkmcnt(2)\n"
      "v_mfma_f32_16x16x32_bf16 v[48:51], v[22:25], v[216:219], v[48:51]\n"
      "s_waitcnt lgkmcnt(0)\n"
      "v_mfma_f32_16x16x32_bf16 v[52:55], v[22:25], v[220:223], v[52:55]\n"
      "s_waitcnt vmcnt(5)\n"
      "s_add_u32 s59, s59, 0x8000\n"
      "s_add_u32 m0, s58, 0x2000\n"
      "s_nop 0\n"
      "buffer_load_dwordx4 v2, s[20:23], s59 offen offset:0 nt lds\n"
      "s_waitcnt vmcnt(5)\n"
      "s_add_u32 m0, s58, 0x2300\n"
      "s_nop 0\n"
      "buffer_load_dwordx4 v3, s[20:23], s59 offen offset:256 nt lds\n"
      "s_waitcnt vmcnt(5)\n"
      "s_add_u32 m0, s58, 0x2600\n"
      "s_nop 0\n"
      "buffer_load_dwordx4 v4, s[20:23], s59 offen offset:512 nt lds\n"
      "s_waitcnt vmcnt(5)\n"
      "s_add_u32 m0, s58, 0x2900\n"
      "s_nop 0\n"
      "buffer_load_dwordx4 v5, s[20:23], s59 offen offset:768 nt lds\n"
      "s_waitcnt vmcnt(5)\n"
      "s_add_u32 m0, s58, 0x2c00\n"
      "s_nop 0\n"
      "buffer_load_dwordx4 v2, s[20:23], s59 offen offset:1024 nt lds\n"
      "s_waitcnt vmcnt(5)\n"
      "s_add_u32 m0, s58, 0x2f00\n"
      "s_nop 0\n"
      "buffer_load_dwordx4 v3, s[20:23], s59 offen offset:1280 nt lds\n"
      "ds_read_b64_tr_b16 v[224:225], v6 offset:0\n"
      "ds_read_b64_tr_b16 v[226:227], v6 offset:4096\n"
      "v_xor_b32 v8, 0x20, v6\n"
      "ds_read_b64_tr_b16 v[228:229], v8 offset:0\n"
      "ds_read_b64_tr_b16 v[230:231], v8 offset:4096\n"
      "v_xor_b32 v7, 0x40, v6\n"
      "ds_read_b64_tr_b16 v[232:233], v7 offset:0\n"
      "ds_read_b64_tr_b16 v[234:235], v7 offset:4096\n"
      "v_xor_b32 v8, 0x60, v6\n"
      "ds_read_b64_tr_b16 v[236:237], v8 offset:0\n"
      "ds_read_b64_tr_b16 v[238:239], v8 offset:4096\n"
      "v_xor_b32 v7, 0x80, v6\n"
      "ds_read_b64_tr_b16 v[240:241], v7 offset:0\n"
      "ds_read_b64_tr_b16 v[242:243], v7 offset:4096\n"
      "v_xor_b32 v8, 0xa0, v6\n"
      "ds_read_b64_tr_b16 v[244:245], v8 offset:0\n"
      "ds_read_b64_tr_b16 v[246:247], v8 offset:4096\n"
      "v_xor_b32 v7, 0xc0, v6\n"
      "ds_read_b64_tr_b16 v[248:249], v7 offset:0\n"
      "ds_read_b64_tr_b16 v[250:251], v7 offset:4096\n"
      "v_xor_b32 v8, 0xe0, v6\n"
      "ds_read_b64_tr_b16 v[252:253], v8 offset:0\n"
      "ds_read_b64_tr_b16 v[254:255], v8 offset:4096\n"
      "s_waitcnt vmcnt(5)\n"
      "s_add_u32 m0, s58, 0x3200\n"
      "s_nop 0\n"
      "buffer_load_dwordx4 v4, s[20:23], s59 offen offset:1536 nt lds\n"
      "s_waitcnt vmcnt(5)\n"
      "s_add_u32 m0, s58, 0x3500\n"
      "s_nop 0\n"
      "buffer_load_dwordx4 v5, s[20:23], s59 offen offset:1792 nt lds\n"
      "v_mov_b32_dpp v10, v38 row_shl:8 row_mask:0xf bank_mask:0x1\n"
      "v_mov_b32_dpp v11, v39 row_shl:8 row_mask:0xf bank_mask:0x1\n"
      "v_mov_b32_dpp v12, v40 row_shl:8 row_mask:0xf bank_mask:0x1\n"
      "v_mov_b32_dpp v13, v41 row_shl:8 row_mask:0xf bank_mask:0x1\n"
      "s_waitcnt lgkmcnt(14)\n"
      "s_nop 0\n"
      "v_mfma_f32_16x16x32_bf16 v[48:51], v[10:13], v[224:227], v[48:51]\n"
      "v_mov_b32_dpp v14, v38 row_shl:4 row_mask:0xf bank_mask:0x2\n"
      "v_mov_b32_dpp v15, v39 row_shl:4 row_mask:0xf bank_mask:0x2\n"
      "v_mov_b32_dpp v16, v40 row_shl:4 row_mask:0xf bank_mask:0x2\n"
      "v_mov_b32_dpp v17, v41 row_shl:4 row_mask:0xf bank_mask:0x2\n"
      "s_waitcnt lgkmcnt(12)\n"
      "v_mfma_f32_16x16x32_bf16 v[52:55], v[10:13], v[228:231], v[52:55]\n"
      "s_waitcnt lgkmcnt(10)\n"
      "v_mfma_f32_16x16x32_bf16 v[48:51], v[14:17], v[232:235], v[48:51]\n"
      "v_mov_b32_dpp v18, v38 quad_perm:[0,1,2,3] row_mask:0xf bank_mask:0x4\n"
      "v_mov_b32_dpp v19, v39 quad_perm:[0,1,2,3] row_mask:0xf bank_mask:0x4\n"
      "v_mov_b32_dpp v20, v40 quad_perm:[0,1,2,3] row_mask:0xf bank_mask:0x4\n"
      "v_mov_b32_dpp v21, v41 quad_perm:[0,1,2,3] row_mask:0xf bank_mask:0x4\n"
      "s_waitcnt lgkmcnt(8)\n"
      "v_mfma_f32_16x16x32_bf16 v[52:55], v[14:17], v[236:239], v[52:55]\n"
      "s_waitcnt lgkmcnt(6)\n"
      "v_mfma_f32_16x16x32_bf16 v[48:51], v[18:21], v[240:243], v[48:51]\n"
      "v_mov_b32_dpp v22, v38 row_shr:4 row_mask:0xf bank_mask:0x8\n"
      "v_mov_b32_dpp v23, v39 row_shr:4 row_mask:0xf bank_mask:0x8\n"
      "v_mov_b32_dpp v24, v40 row_shr:4 row_mask:0xf bank_mask:0x8\n"
      "v_mov_b32_dpp v25, v41 row_shr:4 row_mask:0xf bank_mask:0x8\n"
      "s_waitcnt lgkmcnt(4)\n"
      "v_mfma_f32_16x16x32_bf16 v[52:55], v[18:21], v[244:247], v[52:55]\n"
      "s_waitcnt lgkmcnt(2)\n"
      "v_mfma_f32_16x16x32_bf16 v[48:51], v[22:25], v[248:251], v[48:51]\n"
      "s_waitcnt lgkmcnt(0)\n"
      "v_mfma_f32_16x16x32_bf16 v[52:55], v[22:25], v[252:255], v[52:55]\n"
      "s_waitcnt vmcnt(5)\n"
      "s_add_u32 s59, s59, 0x8000\n"
      "s_add_u32 m0, s58, 0x4000\n"
      "s_nop 0\n"
      "buffer_load_dwordx4 v2, s[20:23], s59 offen offset:0 nt lds\n"
      "s_waitcnt vmcnt(5)\n"
      "s_add_u32 m0, s58, 0x4300\n"
      "s_nop 0\n"
      "buffer_load_dwordx4 v3, s[20:23], s59 offen offset:256 nt lds\n"
      "s_waitcnt vmcnt(5)\n"
      "s_add_u32 m0, s58, 0x4600\n"
      "s_nop 0\n"
      "buffer_load_dwordx4 v4, s[20:23], s59 offen offset:512 nt lds\n"
      "s_waitcnt vmcnt(5)\n"
      "s_add_u32 m0, s58, 0x4900\n"
      "s_nop 0\n"
      "buffer_load_dwordx4 v5, s[20:23], s59 offen offset:768 nt lds\n"
      "s_waitcnt vmcnt(5)\n"
      "s_add_u32 m0, s58, 0x4c00\n"
      "s_nop 0\n"
      "buffer_load_dwordx4 v2, s[20:23], s59 offen offset:1024 nt lds\n"
      "s_waitcnt vmcnt(5)\n"
      "s_add_u32 m0, s58, 0x4f00\n"
      "s_nop 0\n"
      "buffer_load_dwordx4 v3, s[20:23], s59 offen offset:1280 nt lds\n"
      "v_mov_b32_dpp v10, v38 row_shl:10 row_mask:0xf bank_mask:0x1\n"
      "v_mov_b32_dpp v11, v39 row_shl:10 row_mask:0xf bank_mask:0x1\n"
      "v_mov_b32_dpp v12, v40 row_shl:10 row_mask:0xf bank_mask:0x1\n"
      "v_mov_b32_dpp v13, v41 row_shl:10 row_mask:0xf bank_mask:0x1\n"
      "v_mov_b32_dpp v14, v38 row_shl:6 row_mask:0xf bank_mask:0x2\n"
      "v_mov_b32_dpp v15, v39 row_shl:6 row_mask:0xf bank_mask:0x2\n"
      "v_mov_b32_dpp v16, v40 row_shl:6 row_mask:0xf bank_mask:0x2\n"
      "v_mov_b32_dpp v17, v41 row_shl:6 row_mask:0xf bank_mask:0x2\n"
      "v_mov_b32_dpp v18, v38 row_shl:2 row_mask:0xf bank_mask:0x4\n"
      "v_mov_b32_dpp v19, v39 row_shl:2 row_mask:0xf bank_mask:0x4\n"
      "v_mov_b32_dpp v20, v40 row_shl:2 row_mask:0xf bank_mask:0x4\n"
      "v_mov_b32_dpp v21, v41 row_shl:2 row_mask:0xf bank_mask:0x4\n"
      "v_mov_b32_dpp v22, v38 row_shr:2 row_mask:0xf bank_mask:0x8\n"
      "v_mov_b32_dpp v23, v39 row_shr:2 row_mask:0xf bank_mask:0x8\n"
      "v_mov_b32_dpp v24, v40 row_shr:2 row_mask:0xf bank_mask:0x8\n"
      "v_mov_b32_dpp v25, v41 row_shr:2 row_mask:0xf bank_mask:0x8\n"
      "ds_read_b64_tr_b16 v[26:27], v6 offset:8192\n"
      "ds_read_b64_tr_b16 v[28:29], v6 offset:12288\n"
      "v_xor_b32 v8, 0x20, v6\n"
      "ds_read_b64_tr_b16 v[30:31], v8 offset:8192\n"
      "ds_read_b64_tr_b16 v[32:33], v8 offset:12288\n"
      "v_xor_b32 v7, 0x40, v6\n"
      "ds_read_b64_tr_b16 v[34:35], v7 offset:8192\n"
      "ds_read_b64_tr_b16 v[36:37], v7 offset:12288\n"
      "s_waitcnt lgkmcnt(4)\n"
      "v_mfma_f32_16x16x32_bf16 v[48:51], v[10:13], v[26:29], v[48:51]\n"
      "v_xor_b32 v8, 0x60, v6\n"
      "ds_read_b64_tr_b16 v[26:27], v8 offset:8192\n"
      "ds_read_b64_tr_b16 v[28:29], v8 offset:12288\n"
      "s_waitcnt lgkmcnt(4)\n"
      "v_mfma_f32_16x16x32_bf16 v[52:55], v[10:13], v[30:33], v[52:55]\n"
      "v_xor_b32 v7, 0x80, v6\n"
      "ds_read_b64_tr_b16 v[30:31], v7 offset:8192\n"
      "ds_read_b64_tr_b16 v[32:33], v7 offset:12288\n"
      "s_waitcnt lgkmcnt(4)\n"
      "v_mfma_f32_16x16x32_bf16 v[48:51], v[14:17], v[34:37], v[48:51]\n"
      "v_xor_b32 v8, 0xa0, v6\n"
      "ds_read_b64_tr_b16 v[34:35], v8 offset:8192\n"
      "ds_read_b64_tr_b16 v[36:37], v8 offset:12288\n"
      "s_waitcnt lgkmcnt(4)\n"
      "v_mfma_f32_16x16x32_bf16 v[52:55], v[14:17], v[26:29], v[52:55]\n"
      "v_xor_b32 v7, 0xc0, v6\n"
      "ds_read_b64_tr_b16 v[26:27], v7 offset:8192\n"
      "ds_read_b64_tr_b16 v[28:29], v7 offset:12288\n"
      "s_waitcnt lgkmcnt(4)\n"
      "v_mfma_f32_16x16x32_bf16 v[48:51], v[18:21], v[30:33], v[48:51]\n"
      "v_xor_b32 v8, 0xe0, v6\n"
      "ds_read_b64_tr_b16 v[30:31], v8 offset:8192\n"
      "ds_read_b64_tr_b16 v[32:33], v8 offset:12288\n"
      "s_waitcnt lgkmcnt(4)\n"
      "v_mfma_f32_16x16x32_bf16 v[52:55], v[18:21], v[34:37], v[52:55]\n"
      "s_waitcnt lgkmcnt(2)\n"
      "v_mfma_f32_16x16x32_bf16 v[48:51], v[22:25], v[26:29], v[48:51]\n"
      "s_waitcnt lgkmcnt(0)\n"
      "v_mfma_f32_16x16x32_bf16 v[52:55], v[22:25], v[30:33], v[52:55]\n"
      "s_waitcnt vmcnt(5)\n"
      "s_add_u32 m0, s58, 0x5200\n"
      "s_nop 0\n"
      "buffer_load_dwordx4 v4, s[20:23], s59 offen offset:1536 nt lds\n"
      "s_waitcnt vmcnt(5)\n"
      "s_add_u32 m0, s58, 0x5500\n"
      "s_nop 0\n"
      "buffer_load_dwordx4 v5, s[20:23], s59 offen offset:1792 nt lds\n"
      "s_waitcnt vmcnt(5)\n"
      "s_add_u32 s59, s59, 0x8000\n"
      "s_add_u32 m0, s58, 0x0\n"
      "s_nop 0\n"
      "buffer_load_dwordx4 v2, s[20:23], s59 offen offset:0 nt lds\n"
      "s_waitcnt vmcnt(5)\n"
      "s_add_u32 m0, s58, 0x300\n"
      "s_nop 0\n"
      "buffer_load_dwordx4 v3, s[20:23], s59 offen offset:256 nt lds\n"
      "s_waitcnt vmcnt(5)\n"
      "s_add_u32 m0, s58, 0x600\n"
      "s_nop 0\n"
      "buffer_load_dwordx4 v4, s[20:23], s59 offen offset:512 nt lds\n"
      "s_waitcnt vmcnt(5)\n"
      "s_add_u32 m0, s58, 0x900\n"
      "s_nop 0\n"
      "buffer_load_dwordx4 v5, s[20:23], s59 offen offset:768 nt lds\n"
      "s_waitcnt vmcnt(5)\n"
      "s_add_u32 m0, s58, 0xc00\n"
      "s_nop 0\n"
      "buffer_load_dwordx4 v2, s[20:23], s59 offen offset:1024 nt lds\n"
      "s_waitcnt vmcnt(5)\n"
      "s_add_u32 m0, s58, 0xf00\n"
      "s_nop 0\n"
      "buffer_load_dwordx4 v3, s[20:23], s59 offen offset:1280 nt lds\n"
      "v_mov_b32_dpp v10, v38 row_shl:12 row_mask:0xf bank_mask:0x1\n"
      "v_mov_b32_dpp v11, v39 row_shl:12 row_mask:0xf bank_mask:0x1\n"
      "v_mov_b32_dpp v12, v40 row_shl:12 row_mask:0xf bank_mask:0x1\n"
      "v_mov_b32_dpp v13, v41 row_shl:12 row_mask:0xf bank_mask:0x1\n"
      "v_mov_b32_dpp v14, v38 row_shl:8 row_mask:0xf bank_mask:0x2\n"
      "v_mov_b32_dpp v15, v39 row_shl:8 row_mask:0xf bank_mask:0x2\n"
      "v_mov_b32_dpp v16, v40 row_shl:8 row_mask:0xf bank_mask:0x2\n"
      "v_mov_b32_dpp v17, v41 row_shl:8 row_mask:0xf bank_mask:0x2\n"
      "v_mov_b32_dpp v18, v38 row_shl:4 row_mask:0xf bank_mask:0x4\n"
      "v_mov_b32_dpp v19, v39 row_shl:4 row_mask:0xf bank_mask:0x4\n"
      "v_mov_b32_dpp v20, v40 row_shl:4 row_mask:0xf bank_mask:0x4\n"
      "v_mov_b32_dpp v21, v41 row_shl:4 row_mask:0xf bank_mask:0x4\n"
      "v_mov_b32_dpp v22, v38 quad_perm:[0,1,2,3] row_mask:0xf bank_mask:0x8\n"
      "v_mov_b32_dpp v23, v39 quad_perm:[0,1,2,3] row_mask:0xf bank_mask:0x8\n"
      "v_mov_b32_dpp v24, v40 quad_perm:[0,1,2,3] row_mask:0xf bank_mask:0x8\n"
      "v_mov_b32_dpp v25, v41 quad_perm:[0,1,2,3] row_mask:0xf bank_mask:0x8\n"
      "ds_read_b64_tr_b16 v[26:27], v6 offset:16384\n"
      "ds_read_b64_tr_b16 v[28:29], v6 offset:20480\n"
      "v_xor_b32 v8, 0x20, v6\n"
      "ds_read_b64_tr_b16 v[30:31], v8 offset:16384\n"
      "ds_read_b64_tr_b16 v[32:33], v8 offset:20480\n"
      "v_xor_b32 v7, 0x40, v6\n"
      "ds_read_b64_tr_b16 v[34:35], v7 offset:16384\n"
      "ds_read_b64_tr_b16 v[36:37], v7 offset:20480\n"
      "s_waitcnt lgkmcnt(4)\n"
      "v_mfma_f32_16x16x32_bf16 v[48:51], v[10:13], v[26:29], v[48:51]\n"
      "v_xor_b32 v8, 0x60, v6\n"
      "ds_read_b64_tr_b16 v[26:27], v8 offset:16384\n"
      "ds_read_b64_tr_b16 v[28:29], v8 offset:20480\n"
      "s_waitcnt lgkmcnt(4)\n"
      "v_mfma_f32_16x16x32_bf16 v[52:55], v[10:13], v[30:33], v[52:55]\n"
      "v_xor_b32 v7, 0x80, v6\n"
      "ds_read_b64_tr_b16 v[30:31], v7 offset:16384\n"
      "ds_read_b64_tr_b16 v[32:33], v7 offset:20480\n"
      "s_waitcnt lgkmcnt(4)\n"
      "v_mfma_f32_16x16x32_bf16 v[48:51], v[14:17], v[34:37], v[48:51]\n"
      "v_xor_b32 v8, 0xa0, v6\n"
      "ds_read_b64_tr_b16 v[34:35], v8 offset:16384\n"
      "ds_read_b64_tr_b16 v[36:37], v8 offset:20480\n"
      "s_waitcnt lgkmcnt(4)\n"
      "v_mfma_f32_16x16x32_bf16 v[52:55], v[14:17], v[26:29], v[52:55]\n"
      "v_xor_b32 v7, 0xc0, v6\n"
      "ds_read_b64_tr_b16 v[26:27], v7 offset:16384\n"
      "ds_read_b64_tr_b16 v[28:29], v7 offset:20480\n"
      "s_waitcnt lgkmcnt(4)\n"
      "v_mfma_f32_16x16x32_bf16 v[48:51], v[18:21], v[30:33], v[48:51]\n"
      "v_xor_b32 v8, 0xe0, v6\n"
      "ds_read_b64_tr_b16 v[30:31], v8 offset:16384\n"
      "ds_read_b64_tr_b16 v[32:33], v8 offset:20480\n"
      "s_waitcnt lgkmcnt(4)\n"
      "v_mfma_f32_16x16x32_bf16 v[52:55], v[18:21], v[34:37], v[52:55]\n"
      "s_waitcnt lgkmcnt(2)\n"
      "v_mfma_f32_16x16x32_bf16 v[48:51], v[22:25], v[26:29], v[48:51]\n"
      "s_waitcnt lgkmcnt(0)\n"
      "v_mfma_f32_16x16x32_bf16 v[52:55], v[22:25], v[30:33], v[52:55]\n"
      "s_waitcnt vmcnt(5)\n"
      "s_add_u32 m0, s58, 0x1200\n"
      "s_nop 0\n"
      "buffer_load_dwordx4 v4, s[20:23], s59 offen offset:1536 nt lds\n"
      "s_waitcnt vmcnt(5)\n"
      "s_add_u32 m0, s58, 0x1500\n"
      "s_nop 0\n"
      "buffer_load_dwordx4 v5, s[20:23], s59 offen offset:1792 nt lds\n"
      "s_lshr_b32 s61, s12, 3\n"
      "s_and_b32 s62, s12, 7\n"
      "s_mov_b32 s60, 0x0\n"
      "s_mul_i32 s63, s61, 0x28800\n"
      "s_add_u32 s60, s60, s63\n"
      "s_mul_i32 s63, s62, 0x480\n"
      "s_add_u32 s60, s60, s63\n"
      "s_lshr_b32 s61, s13, 1\n"
      "s_mul_i32 s61, s61, 82944\n"
      "s_add_u32 s60, s60, s61\n"
      "v_lshl_add_u32 v26, v0, 4, s60\n"
      "v_lshrrev_b32 v28, 3, v0\n"
      "v_mul_u32_u24 v28, 0x2400, v28\n"
      "v_add_u32 v27, s60, v28\n"
      "v_and_b32 v28, 7, v0\n"
      "v_lshl_add_u32 v27, v28, 4, v27\n"
      "s_bitcmp1_b32 s13, 0\n"
      "s_cselect_b64 s[4:5], s[40:41], s[36:37]\n"
      "s_mov_b64 s[6:7], s[38:39]\n"
      "s_mov_b32 s60, 0x0\n"
      "buffer_load_dwordx4 v[42:45], v26, s[4:7], s60 offen offset:0 nt\n"
      "buffer_load_dwordx4 v[42:45], v26, s[4:7], s60 offen offset:0\n"
      "s_mov_b32 s60, 0x2400\n"
      "buffer_load_dwordx4 v[42:45], v26, s[4:7], s60 offen offset:0 nt\n"
      "buffer_load_dwordx4 v[42:45], v26, s[4:7], s60 offen offset:0\n"
      "s_mov_b32 s60, 0x4800\n"
      "buffer_load_dwordx4 v[42:45], v26, s[4:7], s60 offen offset:0 nt\n"
      "buffer_load_dwordx4 v[42:45], v26, s[4:7], s60 offen offset:0\n"
      "s_mov_b32 s60, 0x6c00\n"
      "buffer_load_dwordx4 v[42:45], v26, s[4:7], s60 offen offset:0 nt\n"
      "buffer_load_dwordx4 v[42:45], v26, s[4:7], s60 offen offset:0\n"
      "s_mov_b32 s60, 0x9000\n"
      "buffer_load_dwordx4 v[42:45], v26, s[4:7], s60 offen offset:0 nt\n"
      "buffer_load_dwordx4 v[42:45], v26, s[4:7], s60 offen offset:0\n"
      "s_mov_b32 s60, 0xb400\n"
      "buffer_load_dwordx4 v[42:45], v26, s[4:7], s60 offen offset:0 nt\n"
      "buffer_load_dwordx4 v[42:45], v26, s[4:7], s60 offen offset:0\n"
      "s_mov_b32 s60, 0xd800\n"
      "buffer_load_dwordx4 v[42:45], v26, s[4:7], s60 offen offset:0 nt\n"
      "buffer_load_dwordx4 v[42:45], v26, s[4:7], s60 offen offset:0\n"
      "s_mov_b32 s60, 0xfc00\n"
      "buffer_load_dwordx4 v[42:45], v26, s[4:7], s60 offen offset:0 nt\n"
      "buffer_load_dwordx4 v[42:45], v26, s[4:7], s60 offen offset:0\n"
      "s_mov_b32 s60, 0x12000\n"
      "buffer_load_dwordx4 v[42:45], v26, s[4:7], s60 offen offset:0 nt\n"
      "buffer_load_dwordx4 v[42:45], v26, s[4:7], s60 offen offset:0\n"
      "buffer_load_dwordx4 v[42:45], v27, s[4:7], 0 offen offset:1024 nt\n"
      "buffer_load_dwordx4 v[42:45], v27, s[4:7], 0 offen offset:1024\n"
      "s_mov_b64 exec, 0xff\n"
      "s_mov_b32 s60, 0x12000\n"
      "buffer_load_dwordx4 v[42:45], v27, s[4:7], s60 offen offset:1024 nt\n"
      "buffer_load_dwordx4 v[42:45], v27, s[4:7], s60 offen offset:1024\n"
      "s_mov_b64 exec, -1\n"
      "s_mov_b32 s60, 0x18000\n"
      "s_mul_i32 s63, s13, 0x100\n"
      "s_add_u32 s60, s60, s63\n"
      "v_lshl_add_u32 v56, v0, 2, s60\n"
      "v_xor_b32 v2, 0x100, v56\n"
      "v_xor_b32 v3, 0x200, v56\n"
      "v_xor_b32 v4, 0x300, v56\n"
      "v_and_b32 v1, 1, v0\n"
      "v_lshlrev_b32 v57, 1, v1\n"
      "v_bfe_u32 v1, v0, 1, 1\n"
      "v_lshl_add_u32 v57, v1, 0, v57\n"
      "v_xor_b32 v1, s13, v57\n"
      "v_lshlrev_b32 v57, 3, v57\n"
      "v_lshl_add_u32 v57, v1, 5, v57\n"
      "v_add_u32 v57, 0x18c00, v57\n"
      "s_mov_b32 s60, 0x18c00\n"
      "s_mul_i32 s63, s13, 0x20\n"
      "s_add_u32 s60, s60, s63\n"
      "v_mov_b32 v5, s60\n"
      "s_lshr_b32 s61, s13, 1\n"
      "s_and_b32 s62, s13, 1\n"
      "s_mov_b32 s60, 0x20000\n"
      "s_mul_i32 s63, s57, 0x8000\n"
      "s_add_u32 s60, s60, s63\n"
      "s_mul_i32 s63, s61, 0x4000\n"
      "s_add_u32 s60, s60, s63\n"
      "s_mul_i32 s63, s62, 0x800\n"
      "s_add_u32 s60, s60, s63\n"
      "s_mul_i32 s63, s69, 0x80\n"
      "s_add_u32 s60, s60, s63\n"
      "v_lshrrev_b32 v1, 4, v0\n"
      "v_lshl_add_u32 v58, v1, 12, s60\n"
      "v_and_b32 v1, 15, v0\n"
      "v_lshl_add_u32 v58, v1, 3, v58\n"
      "s_mov_b32 s60, 0x20000\n"
      "s_mul_i32 s63, s56, 0x800\n"
      "s_add_u32 s60, s60, s63\n"
      "v_and_b32 v1, 3, v0\n"
      "v_lshl_add_u32 v9, v1, 9, s60\n"
      "v_bfe_u32 v1, v0, 2, 2\n"
      "v_lshl_add_u32 v9, v1, 7, v9\n"
      "v_lshrrev_b32 v1, 4, v0\n"
      "v_lshl_add_u32 v9, v1, 5, v9\n"
      "s_mov_b32 s15, 0\n"
      "s_mov_b32 s28, 0\n"
      "s_mov_b32 s29, 0\n"
      "s_mov_b32 s30, 0x20000\n"
      "s_mov_b32 s31, 0x40000\n"
      "s_mov_b32 s32, 0\n"
      "s_mov_b32 s33, 0\n"
      "v_mov_b32_dpp v10, v38 row_shl:14 row_mask:0xf bank_mask:0x1\n"
      "v_mov_b32_dpp v11, v39 row_shl:14 row_mask:0xf bank_mask:0x1\n"
      "v_mov_b32_dpp v12, v40 row_shl:14 row_mask:0xf bank_mask:0x1\n"
      "v_mov_b32_dpp v13, v41 row_shl:14 row_mask:0xf bank_mask:0x1\n"
      "v_mov_b32_dpp v14, v38 row_shl:10 row_mask:0xf bank_mask:0x2\n"
      "v_mov_b32_dpp v15, v39 row_shl:10 row_mask:0xf bank_mask:0x2\n"
      "v_mov_b32_dpp v16, v40 row_shl:10 row_mask:0xf bank_mask:0x2\n"
      "v_mov_b32_dpp v17, v41 row_shl:10 row_mask:0xf bank_mask:0x2\n"
      "v_mov_b32_dpp v18, v38 row_shl:6 row_mask:0xf bank_mask:0x4\n"
      "v_mov_b32_dpp v19, v39 row_shl:6 row_mask:0xf bank_mask:0x4\n"
      "v_mov_b32_dpp v20, v40 row_shl:6 row_mask:0xf bank_mask:0x4\n"
      "v_mov_b32_dpp v21, v41 row_shl:6 row_mask:0xf bank_mask:0x4\n"
      "v_mov_b32_dpp v22, v38 row_shl:2 row_mask:0xf bank_mask:0x8\n"
      "v_mov_b32_dpp v23, v39 row_shl:2 row_mask:0xf bank_mask:0x8\n"
      "v_mov_b32_dpp v24, v40 row_shl:2 row_mask:0xf bank_mask:0x8\n"
      "v_mov_b32_dpp v25, v41 row_shl:2 row_mask:0xf bank_mask:0x8\n"
      "s_waitcnt vmcnt(22)\n"
      "ds_read_b64_tr_b16 v[26:27], v6 offset:0\n"
      "ds_read_b64_tr_b16 v[28:29], v6 offset:4096\n"
      "v_xor_b32 v8, 0x20, v6\n"
      "ds_read_b64_tr_b16 v[30:31], v8 offset:0\n"
      "ds_read_b64_tr_b16 v[32:33], v8 offset:4096\n"
      "v_xor_b32 v7, 0x40, v6\n"
      "ds_read_b64_tr_b16 v[34:35], v7 offset:0\n"
      "ds_read_b64_tr_b16 v[36:37], v7 offset:4096\n"
      "s_waitcnt lgkmcnt(4)\n"
      "v_mfma_f32_16x16x32_bf16 v[48:51], v[10:13], v[26:29], v[48:51]\n"
      "v_xor_b32 v8, 0x60, v6\n"
      "ds_read_b64_tr_b16 v[26:27], v8 offset:0\n"
      "ds_read_b64_tr_b16 v[28:29], v8 offset:4096\n"
      "s_waitcnt lgkmcnt(4)\n"
      "v_mfma_f32_16x16x32_bf16 v[52:55], v[10:13], v[30:33], v[52:55]\n"
      "v_xor_b32 v7, 0x80, v6\n"
      "ds_read_b64_tr_b16 v[30:31], v7 offset:0\n"
      "ds_read_b64_tr_b16 v[32:33], v7 offset:4096\n"
      "s_waitcnt lgkmcnt(4)\n"
      "v_mfma_f32_16x16x32_bf16 v[48:51], v[14:17], v[34:37], v[48:51]\n"
      "v_xor_b32 v8, 0xa0, v6\n"
      "ds_read_b64_tr_b16 v[34:35], v8 offset:0\n"
      "ds_read_b64_tr_b16 v[36:37], v8 offset:4096\n"
      "s_waitcnt lgkmcnt(4)\n"
      "v_mfma_f32_16x16x32_bf16 v[52:55], v[14:17], v[26:29], v[52:55]\n"
      "v_xor_b32 v7, 0xc0, v6\n"
      "ds_read_b64_tr_b16 v[26:27], v7 offset:0\n"
      "ds_read_b64_tr_b16 v[28:29], v7 offset:4096\n"
      "s_waitcnt lgkmcnt(4)\n"
      "v_mfma_f32_16x16x32_bf16 v[48:51], v[18:21], v[30:33], v[48:51]\n"
      "v_xor_b32 v8, 0xe0, v6\n"
      "ds_read_b64_tr_b16 v[30:31], v8 offset:0\n"
      "ds_read_b64_tr_b16 v[32:33], v8 offset:4096\n"
      "s_waitcnt lgkmcnt(4)\n"
      "v_mfma_f32_16x16x32_bf16 v[52:55], v[18:21], v[34:37], v[52:55]\n"
      "s_waitcnt lgkmcnt(2)\n"
      "v_mfma_f32_16x16x32_bf16 v[48:51], v[22:25], v[26:29], v[48:51]\n"
      "s_waitcnt lgkmcnt(0)\n"
      "v_mfma_f32_16x16x32_bf16 v[52:55], v[22:25], v[30:33], v[52:55]\n"
      "s_waitcnt lgkmcnt(0)\n"
      ".Lk_loop:\n"
      "s_nop 6\n"
      "ds_write_b32 v56, v52 offset:0\n"
      "ds_write_b32 v56, v49 offset:1024\n"
      "ds_write_b32 v56, v53 offset:2048\n"
      "s_mov_b64 exec, 0xf\n"
      "ds_write_b64 v57, v[46:47]\n"
      "s_mov_b64 exec, -1\n"
      "s_waitcnt lgkmcnt(0)\n"
      "s_barrier\n"
      "ds_read_b128 v[26:29], v5\n"
      "ds_read_b128 v[30:33], v5 offset:16\n"
      "ds_read_b32 v34, v2 offset:0\n"
      "ds_read_b32 v35, v3 offset:1024\n"
      "ds_read_b32 v36, v4 offset:2048\n"
      "s_mov_b32 vcc_lo, 0xaaaaaaaa\n"
      "s_mov_b32 vcc_hi, 0xaaaaaaaa\n"
      "s_waitcnt lgkmcnt(3)\n"
      "v_max3_f32 v38, v26, v28, v30\n"
      "v_max_f32 v38, v38, v32\n"
      "v_sub_f32 v26, v26, v38\n"
      "v_sub_f32 v28, v28, v38\n"
      "v_sub_f32 v30, v30, v38\n"
      "v_sub_f32 v32, v32, v38\n"
      "v_exp_f32 v26, v26\n"
      "v_exp_f32 v28, v28\n"
      "v_exp_f32 v30, v30\n"
      "v_exp_f32 v32, v32\n"
      "v_mul_f32 v39, v26, v27\n"
      "v_mul_f32 v48, v26, v48\n"
      "v_fmac_f32 v39, v28, v29\n"
      "s_waitcnt lgkmcnt(2)\n"
      "v_fmac_f32 v48, v28, v34\n"
      "v_fmac_f32 v39, v30, v31\n"
      "s_waitcnt lgkmcnt(1)\n"
      "v_fmac_f32 v48, v30, v35\n"
      "v_fmac_f32 v39, v32, v33\n"
      "s_waitcnt lgkmcnt(0)\n"
      "v_fmac_f32 v48, v32, v36\n"
      "v_cndmask_b32 v49, v38, v39, vcc\n"
      "buffer_store_dwordx2 v[48:49], v58, s[24:27], s28 offen sc1\n"
      "s_cmp_lg_u32 s15, 0\n"
      "s_cbranch_scc1 .Lk_wa\n"
      "s_mov_b32 s60, 0x0\n"
      "s_mul_i32 s63, s13, 0x144000\n"
      "s_add_u32 s60, s60, s63\n"
      "s_mul_i32 s63, s56, 0x24\n"
      "s_add_u32 s60, s60, s63\n"
      "v_bfe_u32 v1, v0, 3, 2\n"
      "v_mul_u32_u24 v1, 0x12000, v1\n"
      "v_add_u32 v24, s60, v1\n"
      "v_lshrrev_b32 v1, 5, v0\n"
      "v_mul_u32_u24 v1, 0x9000, v1\n"
      "v_add_u32 v24, v24, v1\n"
      "v_bfe_u32 v1, v0, 1, 2\n"
      "v_mul_u32_u24 v1, 0x2400, v1\n"
      "v_add_u32 v24, v24, v1\n"
      "v_and_b32 v1, 1, v0\n"
      "v_lshl_add_u32 v24, v1, 4, v24\n"
      "s_add_u32 s61, s60, 32\n"
      "v_bfe_u32 v1, v0, 3, 2\n"
      "v_mul_u32_u24 v1, 0x12000, v1\n"
      "v_add_u32 v25, s61, v1\n"
      "v_lshrrev_b32 v1, 5, v0\n"
      "v_mul_u32_u24 v1, 0x9000, v1\n"
      "v_add_u32 v25, v25, v1\n"
      "v_bfe_u32 v1, v0, 1, 2\n"
      "v_mul_u32_u24 v1, 0x2400, v1\n"
      "v_add_u32 v25, v25, v1\n"
      "s_add_u32 s61, s60, 0x120000\n"
      "v_bfe_u32 v1, v0, 1, 4\n"
      "v_mul_u32_u24 v1, 0x2400, v1\n"
      "v_add_u32 v6, s61, v1\n"
      "v_and_b32 v1, 1, v0\n"
      "v_lshl_add_u32 v6, v1, 4, v6\n"
      "s_add_u32 s61, s61, 32\n"
      "v_bfe_u32 v1, v0, 1, 4\n"
      "v_mul_u32_u24 v1, 0x2400, v1\n"
      "v_add_u32 v7, s61, v1\n"
      "s_mov_b32 exec_lo, 0xffffffff\n"
      "s_mov_b32 exec_hi, 0x0\n"
      "s_mov_b32 s60, 0x0\n"
      "s_add_u32 m0, s92, 0x2400\n"
      "s_nop 0\n"
      "buffer_load_dwordx4 v6, s[36:39], s60 offen lds\n"
      "s_mov_b64 exec, 0x55555555\n"
      "s_mov_b32 s60, 0x0\n"
      "s_add_u32 m0, s92, 0x2800\n"
      "s_nop 0\n"
      "buffer_load_dword v7, s[36:39], s60 offen lds\n"
      "s_mov_b32 exec_lo, 0xffffffff\n"
      "s_mov_b32 exec_hi, 0x0\n"
      "s_mov_b32 s60, 0x0\n"
      "s_add_u32 m0, s92, 0x2600\n"
      "s_nop 0\n"
      "buffer_load_dwordx4 v6, s[40:43], s60 offen lds\n"
      "s_mov_b32 exec_lo, 0xaaaaaaaa\n"
      "s_mov_b32 exec_hi, 0x0\n"
      "s_mov_b32 s60, 0x0\n"
      "s_add_u32 m0, s92, 0x2800\n"
      "s_nop 0\n"
      "buffer_load_dword v7, s[40:43], s60 offen lds\n"
      "s_mov_b64 exec, -1\n"
      "s_mov_b32 s60, 0x0\n"
      "s_add_u32 m0, s92, 0x0\n"
      "s_nop 0\n"
      "buffer_load_dwordx4 v24, s[36:39], s60 offen lds\n"
      "s_mov_b32 exec_lo, 0x55555555\n"
      "s_mov_b32 exec_hi, 0x55555555\n"
      "s_mov_b32 s60, 0x0\n"
      "s_add_u32 m0, s92, 0x800\n"
      "s_nop 0\n"
      "buffer_load_dword v25, s[36:39], s60 offen lds\n"
      "s_mov_b64 exec, -1\n"
      "s_mov_b32 s60, 0x0\n"
      "s_add_u32 m0, s92, 0x400\n"
      "s_nop 0\n"
      "buffer_load_dwordx4 v24, s[40:43], s60 offen lds\n"
      "s_mov_b32 exec_lo, 0xaaaaaaaa\n"
      "s_mov_b32 exec_hi, 0xaaaaaaaa\n"
      "s_mov_b32 s60, 0x0\n"
      "s_add_u32 m0, s92, 0x800\n"
      "s_nop 0\n"
      "buffer_load_dword v25, s[40:43], s60 offen lds\n"
      "s_mov_b64 exec, -1\n"
      "s_mov_b32 s60, 0x48000\n"
      "s_add_u32 m0, s92, 0x900\n"
      "s_nop 0\n"
      "buffer_load_dwordx4 v24, s[36:39], s60 offen lds\n"
      "s_mov_b32 exec_lo, 0x55555555\n"
      "s_mov_b32 exec_hi, 0x55555555\n"
      "s_mov_b32 s60, 0x48000\n"
      "s_add_u32 m0, s92, 0x1100\n"
      "s_nop 0\n"
      "buffer_load_dword v25, s[36:39], s60 offen lds\n"
      "s_mov_b64 exec, -1\n"
      "s_mov_b32 s60, 0x48000\n"
      "s_add_u32 m0, s92, 0xd00\n"
      "s_nop 0\n"
      "buffer_load_dwordx4 v24, s[40:43], s60 offen lds\n"
      "s_mov_b32 exec_lo, 0xaaaaaaaa\n"
      "s_mov_b32 exec_hi, 0xaaaaaaaa\n"
      "s_mov_b32 s60, 0x48000\n"
      "s_add_u32 m0, s92, 0x1100\n"
      "s_nop 0\n"
      "buffer_load_dword v25, s[40:43], s60 offen lds\n"
      "s_mov_b64 exec, -1\n"
      "s_mov_b32 s60, 0x90000\n"
      "s_add_u32 m0, s92, 0x1200\n"
      "s_nop 0\n"
      "buffer_load_dwordx4 v24, s[36:39], s60 offen lds\n"
      "s_mov_b32 exec_lo, 0x55555555\n"
      "s_mov_b32 exec_hi, 0x55555555\n"
      "s_mov_b32 s60, 0x90000\n"
      "s_add_u32 m0, s92, 0x1a00\n"
      "s_nop 0\n"
      "buffer_load_dword v25, s[36:39], s60 offen lds\n"
      "s_mov_b64 exec, -1\n"
      "s_mov_b32 s60, 0x90000\n"
      "s_add_u32 m0, s92, 0x1600\n"
      "s_nop 0\n"
      "buffer_load_dwordx4 v24, s[40:43], s60 offen lds\n"
      "s_mov_b32 exec_lo, 0xaaaaaaaa\n"
      "s_mov_b32 exec_hi, 0xaaaaaaaa\n"
      "s_mov_b32 s60, 0x90000\n"
      "s_add_u32 m0, s92, 0x1a00\n"
      "s_nop 0\n"
      "buffer_load_dword v25, s[40:43], s60 offen lds\n"
      "s_mov_b64 exec, -1\n"
      "s_mov_b32 s60, 0xd8000\n"
      "s_add_u32 m0, s92, 0x1b00\n"
      "s_nop 0\n"
      "buffer_load_dwordx4 v24, s[36:39], s60 offen lds\n"
      "s_mov_b32 exec_lo, 0x55555555\n"
      "s_mov_b32 exec_hi, 0x55555555\n"
      "s_mov_b32 s60, 0xd8000\n"
      "s_add_u32 m0, s92, 0x2300\n"
      "s_nop 0\n"
      "buffer_load_dword v25, s[36:39], s60 offen lds\n"
      "s_mov_b64 exec, -1\n"
      "s_mov_b32 s60, 0xd8000\n"
      "s_add_u32 m0, s92, 0x1f00\n"
      "s_nop 0\n"
      "buffer_load_dwordx4 v24, s[40:43], s60 offen lds\n"
      "s_mov_b32 exec_lo, 0xaaaaaaaa\n"
      "s_mov_b32 exec_hi, 0xaaaaaaaa\n"
      "s_mov_b32 s60, 0xd8000\n"
      "s_add_u32 m0, s92, 0x2300\n"
      "s_nop 0\n"
      "buffer_load_dword v25, s[40:43], s60 offen lds\n"
      "s_mov_b64 exec, -1\n"
      ".Lk_wa:\n"
      "s_mov_b32 s60, 0xa0000\n"
      "s_mul_i32 s63, s13, 0x48000\n"
      "s_add_u32 s60, s60, s63\n"
      "s_mul_i32 s63, s12, 0x80\n"
      "s_add_u32 s60, s60, s63\n"
      "v_bfe_u32 v1, v0, 2, 2\n"
      "v_lshl_add_u32 v3, v1, 15, s60\n"
      "v_and_b32 v1, 3, v0\n"
      "v_lshl_add_u32 v3, v1, 5, v3\n"
      "v_lshrrev_b32 v1, 4, v0\n"
      "v_lshl_add_u32 v3, v1, 3, v3\n"
      "s_sleep 4\n"
      "buffer_load_dwordx4 v[26:29], v9, s[24:27], s28 offen sc1\n"
      "buffer_load_dwordx4 v[30:33], v9, s[24:27], s28 offen offset:16 sc1\n"
      "s_waitcnt vmcnt(1)\n"
      "v_max_u32 v1, v26, v27\n"
      "v_max3_u32 v1, v1, v28, v29\n"
      "s_waitcnt vmcnt(0)\n"
      "v_max3_u32 v1, v1, v30, v31\n"
      "v_max3_u32 v1, v1, v32, v33\n"
      "v_cmp_eq_u32 vcc, -1, v1\n"
      "s_cbranch_vccz .Lk_ok\n"
      "s_waitcnt vmcnt(0) lgkmcnt(0)\n"
      "s_mov_b32 s68, 0x8000\n"
      ".Lk_oh:\n"
      "s_sleep 1\n"
      "buffer_load_dwordx4 v[26:29], v9, s[24:27], s28 offen sc1\n"
      "buffer_load_dwordx4 v[30:33], v9, s[24:27], s28 offen offset:16 sc1\n"
      "s_waitcnt vmcnt(1)\n"
      "v_max_u32 v1, v26, v27\n"
      "v_max3_u32 v1, v1, v28, v29\n"
      "s_waitcnt vmcnt(0)\n"
      "v_max3_u32 v1, v1, v30, v31\n"
      "v_max3_u32 v1, v1, v32, v33\n"
      "v_cmp_eq_u32 vcc, -1, v1\n"
      "s_cbranch_vccz .Lk_ok\n"
      "s_sub_u32 s68, s68, 1\n"
      "s_nop 1\n"
      "s_cbranch_scc0 .Lk_oh\n"
      ".Lk_ok:\n"
      "v_max_f32_dpp v34, v27, v27 row_ror:4 row_mask:0xf bank_mask:0xf\n"
      "s_nop 1\n"
      "v_max_f32_dpp v34, v34, v34 row_ror:8 row_mask:0xf bank_mask:0xf\n"
      "v_sub_f32 v35, v27, v34\n"
      "v_exp_f32 v35, v35\n"
      "s_nop 0\n"
      "v_mul_f32 v29, v29, v35\n"
      "v_mul_f32 v26, v26, v35\n"
      "v_mul_f32 v28, v28, v35\n"
      "v_mul_f32 v30, v30, v35\n"
      "v_mul_f32 v32, v32, v35\n"
      "v_add_f32_dpp v29, v29, v29 row_ror:4 row_mask:0xf bank_mask:0xf\n"
      "v_add_f32_dpp v26, v26, v26 row_ror:4 row_mask:0xf bank_mask:0xf\n"
      "v_add_f32_dpp v28, v28, v28 row_ror:4 row_mask:0xf bank_mask:0xf\n"
      "v_add_f32_dpp v30, v30, v30 row_ror:4 row_mask:0xf bank_mask:0xf\n"
      "v_add_f32_dpp v32, v32, v32 row_ror:4 row_mask:0xf bank_mask:0xf\n"
      "v_add_f32_dpp v29, v29, v29 row_ror:8 row_mask:0xf bank_mask:0xf\n"
      "v_add_f32_dpp v26, v26, v26 row_ror:8 row_mask:0xf bank_mask:0xf\n"
      "v_add_f32_dpp v28, v28, v28 row_ror:8 row_mask:0xf bank_mask:0xf\n"
      "v_add_f32_dpp v30, v30, v30 row_ror:8 row_mask:0xf bank_mask:0xf\n"
      "v_add_f32_dpp v32, v32, v32 row_ror:8 row_mask:0xf bank_mask:0xf\n"
      "v_rcp_f32 v35, v29\n"
      "s_nop 0\n"
      "v_mul_f32 v26, v26, v35\n"
      "v_mul_f32 v28, v28, v35\n"
      "v_mul_f32 v30, v30, v35\n"
      "v_mul_f32 v32, v32, v35\n"
      "v_cvt_pk_bf16_f32 v26, v26, v28\n"
      "v_cvt_pk_bf16_f32 v27, v30, v32\n"
      "s_nop 0\n"
      "v_mov_b32_dpp v10, v26 quad_perm:[0,1,2,3] row_mask:0xf bank_mask:0x1\n"
      "v_mov_b32_dpp v11, v27 quad_perm:[0,1,2,3] row_mask:0xf bank_mask:0x1\n"
      "s_nop 1\n"
      "v_mfma_f32_16x16x16_bf16 v[36:39], v[94:95], v[10:11], 0\n"
      "v_mov_b32_dpp v14, v26 quad_perm:[0,1,2,3] row_mask:0xf bank_mask:0x2\n"
      "v_mov_b32_dpp v15, v27 quad_perm:[0,1,2,3] row_mask:0xf bank_mask:0x2\n"
      "v_mfma_f32_16x16x16_bf16 v[40:43], v[78:79], v[10:11], 0\n"
      "v_mov_b32_dpp v18, v26 quad_perm:[0,1,2,3] row_mask:0xf bank_mask:0x4\n"
      "v_mov_b32_dpp v19, v27 quad_perm:[0,1,2,3] row_mask:0xf bank_mask:0x4\n"
      "v_mfma_f32_16x16x16_bf16 v[40:43], v[80:81], v[14:15], v[40:43]\n"
      "v_mov_b32_dpp v22, v26 quad_perm:[0,1,2,3] row_mask:0xf bank_mask:0x8\n"
      "v_mov_b32_dpp v23, v27 quad_perm:[0,1,2,3] row_mask:0xf bank_mask:0x8\n"
      "v_mfma_f32_16x16x16_bf16 v[40:43], v[82:83], v[18:19], v[40:43]\n"
      "s_nop 0\n"
      "v_mfma_f32_16x16x16_bf16 v[40:43], v[84:85], v[22:23], v[40:43]\n"
      "v_mfma_f32_16x16x16_bf16 v[44:47], v[86:87], v[10:11], 0\n"
      "v_mfma_f32_16x16x16_bf16 v[44:47], v[88:89], v[14:15], v[44:47]\n"
      "v_cvt_pk_bf16_f32 v36, v36, v37\n"
      "v_cvt_pk_bf16_f32 v37, v38, v39\n"
      "s_mov_b64 exec, s[64:65]\n"
      "buffer_store_dwordx2 v[36:37], v3, s[24:27], s31 offen sc1\n"
      "s_mov_b64 exec, -1\n"
      "v_mfma_f32_16x16x16_bf16 v[44:47], v[90:91], v[18:19], v[44:47]\n"
      "v_mfma_f32_16x16x16_bf16 v[44:47], v[92:93], v[22:23], v[44:47]\n"
      "v_cvt_pk_bf16_f32 v40, v40, v41\n"
      "v_cvt_pk_bf16_f32 v41, v42, v43\n"
      "buffer_store_dwordx2 v[40:41], v3, s[24:27], s29 offen sc1\n"
      "s_nop 4\n"
      "v_cvt_pk_bf16_f32 v44, v44, v45\n"
      "v_cvt_pk_bf16_f32 v45, v46, v47\n"
      "buffer_store_dwordx2 v[44:45], v3, s[24:27], s30 offen sc1\n"
      "s_barrier\n"
      "s_cmp_ge_u32 s12, 36\n"
      "s_cbranch_scc1 .Lk_rx\n"
      "s_mov_b32 s60, 0xa0000\n"
      "s_mul_i32 s63, s12, 0x8000\n"
      "s_add_u32 s60, s60, s63\n"
      "s_mul_i32 s63, s13, 0x2000\n"
      "s_add_u32 s60, s60, s63\n"
      "v_lshl_add_u32 v2, v0, 4, s60\n"
      "v_add_u32 v4, 0x1000, v2\n"
      "v_and_b32 v1, 15, v0\n"
      "v_mov_b32 v10, 0\n"
      "v_cmp_eq_u32 vcc, 0, v1\n"
      "v_mov_b32 v5, 0x3f80\n"
      "s_nop 0\n"
      "v_cndmask_b32 v10, v10, v5, vcc\n"
      "v_cmp_eq_u32 vcc, 1, v1\n"
      "v_mov_b32 v5, 1.0\n"
      "s_nop 0\n"
      "v_cndmask_b32 v10, v10, v5, vcc\n"
      "v_mov_b32 v11, 0\n"
      "v_cmp_eq_u32 vcc, 2, v1\n"
      "v_mov_b32 v5, 0x3f80\n"
      "s_nop 0\n"
      "v_cndmask_b32 v11, v11, v5, vcc\n"
      "v_cmp_eq_u32 vcc, 3, v1\n"
      "v_mov_b32 v5, 1.0\n"
      "s_nop 0\n"
      "v_cndmask_b32 v11, v11, v5, vcc\n"
      "v_mov_b32 v12, 0\n"
      "v_cmp_eq_u32 vcc, 4, v1\n"
      "v_mov_b32 v5, 0x3f80\n"
      "s_nop 0\n"
      "v_cndmask_b32 v12, v12, v5, vcc\n"
      "v_cmp_eq_u32 vcc, 5, v1\n"
      "v_mov_b32 v5, 1.0\n"
      "s_nop 0\n"
      "v_cndmask_b32 v12, v12, v5, vcc\n"
      "v_mov_b32 v13, 0\n"
      "v_cmp_eq_u32 vcc, 6, v1\n"
      "v_mov_b32 v5, 0x3f80\n"
      "s_nop 0\n"
      "v_cndmask_b32 v13, v13, v5, vcc\n"
      "v_cmp_eq_u32 vcc, 7, v1\n"
      "v_mov_b32 v5, 1.0\n"
      "s_nop 0\n"
      "v_cndmask_b32 v13, v13, v5, vcc\n"
      "s_mov_b32 s60, 0x12000\n"
      "s_mul_i32 s63, s12, 0x80\n"
      "s_add_u32 s60, s60, s63\n"
      "v_and_b32 v1, 7, v0\n"
      "v_lshl_add_u32 v5, v1, 4, s60\n"
      "v_bfe_u32 v1, v0, 4, 1\n"
      "v_lshl_add_u32 v5, v1, 3, v5\n"
      "s_mov_b32 s60, 0x0\n"
      "s_mul_i32 s63, s12, 0x20\n"
      "s_add_u32 s60, s60, s63\n"
      "v_bfe_u32 v1, v0, 1, 2\n"
      "v_mul_u32_u24 v1, 0x480, v1\n"
      "v_add_u32 v6, s60, v1\n"
      "v_and_b32 v1, 1, v0\n"
      "v_lshl_add_u32 v6, v1, 4, v6\n"
      "v_bfe_u32 v1, v0, 4, 1\n"
      "v_lshl_add_u32 v6, v1, 3, v6\n"
      "s_mov_b32 s60, 0x17c00\n"
      "s_mul_i32 s63, s13, 0x400\n"
      "s_add_u32 s60, s60, s63\n"
      "v_and_b32 v1, 7, v0\n"
      "v_lshl_add_u32 v7, v1, 4, s60\n"
      "v_bfe_u32 v1, v0, 4, 1\n"
      "v_lshl_add_u32 v7, v1, 7, v7\n"
      "s_mov_b64 exec, 0xff00ff\n"
      "s_cmp_eq_u32 s15, 0\n"
      "s_cbranch_scc0 .Lk_rb\n"
      "buffer_load_dwordx2 v[18:19], v6, s[48:51], 0 offen\n"
      "s_branch .Lk_rc\n"
      ".Lk_rb:\n"
      "buffer_load_dwordx2 v[18:19], v5, s[24:27], s33 offen sc1\n"
      ".Lk_rc:\n"
      "s_mov_b64 exec, -1\n"
      "s_sleep 4\n"
      "buffer_load_dwordx4 v[26:29], v2, s[24:27], s29 offen offset:0 sc1\n"
      "buffer_load_dwordx4 v[30:33], v2, s[24:27], s29 offen offset:1024 sc1\n"
      "buffer_load_dwordx4 v[34:37], v2, s[24:27], s29 offen offset:2048 sc1\n"
      "buffer_load_dwordx4 v[38:41], v2, s[24:27], s29 offen offset:3072 sc1\n"
      "buffer_load_dwordx4 v[42:45], v4, s[24:27], s29 offen offset:0 sc1\n"
      "buffer_load_dwordx4 v[46:49], v4, s[24:27], s29 offen offset:1024 sc1\n"
      "buffer_load_dwordx4 v[50:53], v4, s[24:27], s29 offen offset:2048 sc1\n"
      "buffer_load_dwordx4 v[54:57], v4, s[24:27], s29 offen offset:3072 sc1\n"
      "s_waitcnt vmcnt(7)\n"
      "v_max_u32 v1, v26, v27\n"
      "v_max3_u32 v1, v1, v28, v29\n"
      "s_waitcnt vmcnt(6)\n"
      "v_max3_u32 v1, v1, v30, v31\n"
      "v_max3_u32 v1, v1, v32, v33\n"
      "s_waitcnt vmcnt(5)\n"
      "v_max3_u32 v1, v1, v34, v35\n"
      "v_max3_u32 v1, v1, v36, v37\n"
      "s_waitcnt vmcnt(4)\n"
      "v_max3_u32 v1, v1, v38, v39\n"
      "v_max3_u32 v1, v1, v40, v41\n"
      "s_waitcnt vmcnt(3)\n"
      "v_max3_u32 v1, v1, v42, v43\n"
      "v_max3_u32 v1, v1, v44, v45\n"
      "s_waitcnt vmcnt(2)\n"
      "v_max3_u32 v1, v1, v46, v47\n"
      "v_max3_u32 v1, v1, v48, v49\n"
      "s_waitcnt vmcnt(1)\n"
      "v_max3_u32 v1, v1, v50, v51\n"
      "v_max3_u32 v1, v1, v52, v53\n"
      "s_waitcnt vmcnt(0)\n"
      "v_max3_u32 v1, v1, v54, v55\n"
      "v_max3_u32 v1, v1, v56, v57\n"
      "v_cmp_eq_u32 vcc, -1, v1\n"
      "s_cbranch_vccz .Lk_rk\n"
      "s_waitcnt vmcnt(0) lgkmcnt(0)\n"
      "s_mov_b32 s68, 0x8000\n"
      ".Lk_rh:\n"
      "s_sleep 1\n"
      "buffer_load_dwordx4 v[26:29], v2, s[24:27], s29 offen offset:0 sc1\n"
      "buffer_load_dwordx4 v[30:33], v2, s[24:27], s29 offen offset:1024 sc1\n"
      "buffer_load_dwordx4 v[34:37], v2, s[24:27], s29 offen offset:2048 sc1\n"
      "buffer_load_dwordx4 v[38:41], v2, s[24:27], s29 offen offset:3072 sc1\n"
      "buffer_load_dwordx4 v[42:45], v4, s[24:27], s29 offen offset:0 sc1\n"
      "buffer_load_dwordx4 v[46:49], v4, s[24:27], s29 offen offset:1024 sc1\n"
      "buffer_load_dwordx4 v[50:53], v4, s[24:27], s29 offen offset:2048 sc1\n"
      "buffer_load_dwordx4 v[54:57], v4, s[24:27], s29 offen offset:3072 sc1\n"
      "s_waitcnt vmcnt(7)\n"
      "v_max_u32 v1, v26, v27\n"
      "v_max3_u32 v1, v1, v28, v29\n"
      "s_waitcnt vmcnt(6)\n"
      "v_max3_u32 v1, v1, v30, v31\n"
      "v_max3_u32 v1, v1, v32, v33\n"
      "s_waitcnt vmcnt(5)\n"
      "v_max3_u32 v1, v1, v34, v35\n"
      "v_max3_u32 v1, v1, v36, v37\n"
      "s_waitcnt vmcnt(4)\n"
      "v_max3_u32 v1, v1, v38, v39\n"
      "v_max3_u32 v1, v1, v40, v41\n"
      "s_waitcnt vmcnt(3)\n"
      "v_max3_u32 v1, v1, v42, v43\n"
      "v_max3_u32 v1, v1, v44, v45\n"
      "s_waitcnt vmcnt(2)\n"
      "v_max3_u32 v1, v1, v46, v47\n"
      "v_max3_u32 v1, v1, v48, v49\n"
      "s_waitcnt vmcnt(1)\n"
      "v_max3_u32 v1, v1, v50, v51\n"
      "v_max3_u32 v1, v1, v52, v53\n"
      "s_waitcnt vmcnt(0)\n"
      "v_max3_u32 v1, v1, v54, v55\n"
      "v_max3_u32 v1, v1, v56, v57\n"
      "v_cmp_eq_u32 vcc, -1, v1\n"
      "s_cbranch_vccz .Lk_rk\n"
      "s_sub_u32 s68, s68, 1\n"
      "s_nop 1\n"
      "s_cbranch_scc0 .Lk_rh\n"
      ".Lk_rk:\n"
      "v_mfma_f32_16x16x32_bf16 v[14:17], v[10:13], v[26:29], 0\n"
      "v_mfma_f32_16x16x32_bf16 v[14:17], v[10:13], v[30:33], v[14:17]\n"
      "v_mfma_f32_16x16x32_bf16 v[14:17], v[10:13], v[34:37], v[14:17]\n"
      "v_mfma_f32_16x16x32_bf16 v[14:17], v[10:13], v[38:41], v[14:17]\n"
      "v_mfma_f32_16x16x32_bf16 v[14:17], v[10:13], v[42:45], v[14:17]\n"
      "v_mfma_f32_16x16x32_bf16 v[14:17], v[10:13], v[46:49], v[14:17]\n"
      "v_mfma_f32_16x16x32_bf16 v[14:17], v[10:13], v[50:53], v[14:17]\n"
      "v_mfma_f32_16x16x32_bf16 v[14:17], v[10:13], v[54:57], v[14:17]\n"
      "s_nop 7\n"
      "v_add_f32_dpp v14, v14, v14 row_ror:8 row_mask:0xf bank_mask:0xf\n"
      "v_add_f32_dpp v15, v15, v15 row_ror:8 row_mask:0xf bank_mask:0xf\n"
      "v_add_f32_dpp v16, v16, v16 row_ror:8 row_mask:0xf bank_mask:0xf\n"
      "v_add_f32_dpp v17, v17, v17 row_ror:8 row_mask:0xf bank_mask:0xf\n"
      "s_mov_b64 exec, 0xff00ff\n"
      "s_cmp_eq_u32 s13, 0\n"
      "s_cbranch_scc1 .Lk_rz\n"
      "ds_write_b128 v7, v[14:17]\n"
      "s_waitcnt lgkmcnt(0)\n"
      "s_barrier\n"
      "s_barrier\n"
      "s_branch .Lk_re\n"
      ".Lk_rz:\n"
      "s_barrier\n"
      "ds_read_b128 v[26:29], v7 offset:1024\n"
      "ds_read_b128 v[30:33], v7 offset:2048\n"
      "ds_read_b128 v[34:37], v7 offset:3072\n"
      "v_lshlrev_b32 v20, 16, v18\n"
      "v_and_b32 v21, 0xffff0000, v18\n"
      "v_lshlrev_b32 v22, 16, v19\n"
      "v_and_b32 v23, 0xffff0000, v19\n"
      "s_waitcnt lgkmcnt(0)\n"
      "s_barrier\n"
      "v_pk_add_f32 v[14:15], v[14:15], v[26:27]\n"
      "v_pk_add_f32 v[16:17], v[16:17], v[28:29]\n"
      "v_pk_add_f32 v[14:15], v[14:15], v[30:31]\n"
      "v_pk_add_f32 v[16:17], v[16:17], v[32:33]\n"
      "v_pk_add_f32 v[14:15], v[14:15], v[34:35]\n"
      "v_pk_add_f32 v[16:17], v[16:17], v[36:37]\n"
      "v_cvt_pk_bf16_f32 v18, v14, v15\n"
      "v_cvt_pk_bf16_f32 v19, v16, v17\n"
      "v_lshlrev_b32 v14, 16, v18\n"
      "v_and_b32 v15, 0xffff0000, v18\n"
      "v_lshlrev_b32 v16, 16, v19\n"
      "v_and_b32 v17, 0xffff0000, v19\n"
      "v_pk_add_f32 v[14:15], v[14:15], v[20:21]\n"
      "v_pk_add_f32 v[16:17], v[16:17], v[22:23]\n"
      "v_cvt_pk_bf16_f32 v18, v14, v15\n"
      "v_cvt_pk_bf16_f32 v19, v16, v17\n"
      "buffer_store_dwordx2 v[18:19], v5, s[24:27], s32 offen sc1\n"
      ".Lk_re:\n"
      "s_mov_b64 exec, -1\n"
      ".Lk_rx:\n"
      "s_add_u32 s29, s29, 0x120000\n"
      "s_add_u32 s30, s30, 0x120000\n"
      "s_add_u32 s31, s31, 0x120000\n"
      "s_cmp_lg_u32 s15, 0\n"
      "s_cbranch_scc1 .Lk_wb\n"
      "v_mov_b32 v2, -1\n"
      "v_mov_b32 v3, -1\n"
      "v_mov_b32 v4, -1\n"
      "v_mov_b32 v5, -1\n"
      "s_mov_b32 s60, 0xa000\n"
      "s_mul_i32 s63, s12, 0x1000\n"
      "s_add_u32 s60, s60, s63\n"
      "s_mul_i32 s63, s13, 0x400\n"
      "s_add_u32 s60, s60, s63\n"
      "v_lshl_add_u32 v6, v0, 4, s60\n"
      "s_mov_b64 s[4:5], s[24:25]\n"
      "s_mov_b32 s6, 0x5a0000\n"
      "s_mov_b32 s7, 0x20000\n"
      "s_mov_b32 s60, 0x300000\n"
      "buffer_store_dwordx4 v[2:5], v6, s[4:7], s60 offen sc1\n"
      "s_mov_b32 s60, 0x400000\n"
      "buffer_store_dwordx4 v[2:5], v6, s[4:7], s60 offen sc1\n"
      "s_mov_b32 s60, 0x500000\n"
      "buffer_store_dwordx4 v[2:5], v6, s[4:7], s60 offen sc1\n"
      "v_lshl_add_u32 v54, v0, 3, s92\n"
      "s_add_u32 s60, s92, 2048\n"
      "v_lshrrev_b32 v1, 2, v0\n"
      "v_lshl_add_u32 v55, v1, 3, s60\n"
      "s_mov_b32 s60, 0x18000\n"
      "s_mul_i32 s63, s13, 0x100\n"
      "s_add_u32 s60, s60, s63\n"
      "v_lshrrev_b32 v1, 4, v0\n"
      "v_lshl_add_u32 v56, v1, 6, s60\n"
      "v_and_b32 v1, 3, v0\n"
      "v_lshl_add_u32 v56, v1, 4, v56\n"
      "s_mov_b32 s60, 0x18000\n"
      "v_and_b32 v1, 3, v0\n"
      "v_lshl_add_u32 v57, v1, 4, s60\n"
      "v_bfe_u32 v1, v0, 2, 2\n"
      "v_lshl_add_u32 v57, v1, 2, v57\n"
      "v_add_u32 v58, 0x800, v57\n"
      "v_lshrrev_b32 v1, 4, v0\n"
      "v_lshl_add_u32 v57, v1, 6, v57\n"
      "s_mov_b32 s60, 0x18840\n"
      "v_and_b32 v1, 3, v0\n"
      "v_mul_u32_u24 v1, 0x14, v1\n"
      "v_add_u32 v18, s60, v1\n"
      "v_mov_b32 v2, 0x3f803f80\n"
      "v_mov_b32 v3, 0x3f803f80\n"
      "v_mov_b32 v4, 0x3f803f80\n"
      "v_mov_b32 v5, 0x3f803f80\n"
      "ds_read_b64_tr_b16 v[34:35], v54 offset:9216\n"
      "ds_read_b64_tr_b16 v[18:19], v54 offset:0\n"
      "ds_read_b64_tr_b16 v[20:21], v54 offset:512\n"
      "ds_read_b64_tr_b16 v[22:23], v54 offset:2304\n"
      "ds_read_b64_tr_b16 v[24:25], v54 offset:2816\n"
      "ds_read_b64_tr_b16 v[26:27], v54 offset:4608\n"
      "ds_read_b64_tr_b16 v[28:29], v54 offset:5120\n"
      "ds_read_b64_tr_b16 v[30:31], v54 offset:6912\n"
      "ds_read_b64_tr_b16 v[32:33], v54 offset:7424\n"
      "ds_read_b64_tr_b16 v[52:53], v54 offset:9728\n"
      "ds_read_b64_tr_b16 v[36:37], v54 offset:1024\n"
      "ds_read_b64_tr_b16 v[38:39], v54 offset:1536\n"
      "ds_read_b64_tr_b16 v[40:41], v54 offset:3328\n"
      "ds_read_b64_tr_b16 v[42:43], v54 offset:3840\n"
      "ds_read_b64_tr_b16 v[44:45], v54 offset:5632\n"
      "ds_read_b64_tr_b16 v[46:47], v54 offset:6144\n"
      "ds_read_b64_tr_b16 v[48:49], v54 offset:7936\n"
      "ds_read_b64_tr_b16 v[50:51], v54 offset:8448\n"
      "s_waitcnt lgkmcnt(15)\n"
      "v_mfma_f32_16x16x16_bf16 v[6:9], v[34:35], v[2:3], 0\n"
      "s_nop 5\n"
      "v_mfma_f32_16x16x32_bf16 v[6:9], v[18:21], v[2:5], v[6:9]\n"
      "s_waitcnt lgkmcnt(13)\n"
      "v_mfma_f32_16x16x32_bf16 v[6:9], v[22:25], v[2:5], v[6:9]\n"
      "s_waitcnt lgkmcnt(11)\n"
      "v_mfma_f32_16x16x32_bf16 v[6:9], v[26:29], v[2:5], v[6:9]\n"
      "s_waitcnt lgkmcnt(9)\n"
      "v_mfma_f32_16x16x32_bf16 v[6:9], v[30:33], v[2:5], v[6:9]\n"
      "ds_read_b64_tr_b16 v[34:35], v55 offset:8192\n"
      "ds_read_b64_tr_b16 v[18:19], v55 offset:0\n"
      "ds_read_b64_tr_b16 v[20:21], v55 offset:128\n"
      "ds_read_b64_tr_b16 v[22:23], v55 offset:2304\n"
      "ds_read_b64_tr_b16 v[24:25], v55 offset:2432\n"
      "ds_read_b64_tr_b16 v[26:27], v55 offset:4608\n"
      "ds_read_b64_tr_b16 v[28:29], v55 offset:4736\n"
      "ds_read_b64_tr_b16 v[30:31], v55 offset:6912\n"
      "ds_read_b64_tr_b16 v[32:33], v55 offset:7040\n"
      "s_waitcnt lgkmcnt(15)\n"
      "v_mfma_f32_16x16x16_bf16 v[10:13], v[52:53], v[2:3], 0\n"
      "s_nop 5\n"
      "v_mfma_f32_16x16x32_bf16 v[10:13], v[36:39], v[2:5], v[10:13]\n"
      "s_waitcnt lgkmcnt(13)\n"
      "v_mfma_f32_16x16x32_bf16 v[10:13], v[40:43], v[2:5], v[10:13]\n"
      "s_waitcnt lgkmcnt(11)\n"
      "v_mfma_f32_16x16x32_bf16 v[10:13], v[44:47], v[2:5], v[10:13]\n"
      "s_waitcnt lgkmcnt(9)\n"
      "v_mfma_f32_16x16x32_bf16 v[10:13], v[48:51], v[2:5], v[10:13]\n"
      "s_waitcnt lgkmcnt(8)\n"
      "v_mfma_f32_16x16x16_bf16 v[14:17], v[34:35], v[2:3], 0\n"
      "s_waitcnt lgkmcnt(6)\n"
      "s_nop 4\n"
      "v_mfma_f32_16x16x32_bf16 v[14:17], v[18:21], v[2:5], v[14:17]\n"
      "s_waitcnt lgkmcnt(4)\n"
      "v_mfma_f32_16x16x32_bf16 v[14:17], v[22:25], v[2:5], v[14:17]\n"
      "s_waitcnt lgkmcnt(2)\n"
      "v_mfma_f32_16x16x32_bf16 v[14:17], v[26:29], v[2:5], v[14:17]\n"
      "s_waitcnt lgkmcnt(0)\n"
      "v_mfma_f32_16x16x32_bf16 v[14:17], v[30:33], v[2:5], v[14:17]\n"
      "s_mov_b64 exec, s[64:65]\n"
      "ds_write_b128 v56, v[6:9] offset:0\n"
      "ds_write_b128 v56, v[10:13] offset:1024\n"
      "s_nop 4\n"
      "ds_write_b128 v56, v[14:17] offset:2048\n"
      "s_mov_b64 exec, -1\n"
      "s_waitcnt lgkmcnt(0)\n"
      "s_barrier\n"
      "ds_read2st64_b32 v[18:19], v57 offset0:0 offset1:1\n"
      "ds_read2st64_b32 v[20:21], v57 offset0:2 offset1:3\n"
      "ds_read2st64_b32 v[22:23], v57 offset0:4 offset1:5\n"
      "ds_read2st64_b32 v[24:25], v57 offset0:6 offset1:7\n"
      "ds_read2st64_b32 v[26:27], v58 offset0:0 offset1:1\n"
      "ds_read2st64_b32 v[28:29], v58 offset0:2 offset1:3\n"
      "s_waitcnt lgkmcnt(5)\n"
      "v_add_f32 v18, v18, v19\n"
      "s_waitcnt lgkmcnt(4)\n"
      "v_add_f32 v20, v20, v21\n"
      "v_add_f32 v18, v18, v20\n"
      "s_mov_b32 exec_lo, 0x11111111\n"
      "s_mov_b32 exec_hi, 0x11111111\n"
      "v_mov_b32 v59, v18\n"
      "s_mov_b64 exec, -1\n"
      "s_waitcnt lgkmcnt(3)\n"
      "v_add_f32 v22, v22, v23\n"
      "s_waitcnt lgkmcnt(2)\n"
      "v_add_f32 v24, v24, v25\n"
      "v_add_f32 v22, v22, v24\n"
      "s_mov_b32 exec_lo, 0x22222222\n"
      "s_mov_b32 exec_hi, 0x22222222\n"
      "v_mov_b32 v59, v22\n"
      "s_mov_b64 exec, -1\n"
      "s_waitcnt lgkmcnt(1)\n"
      "v_add_f32 v26, v26, v27\n"
      "s_waitcnt lgkmcnt(0)\n"
      "v_add_f32 v28, v28, v29\n"
      "v_add_f32 v26, v26, v28\n"
      "s_mov_b32 exec_lo, 0x88888888\n"
      "s_mov_b32 exec_hi, 0x88888888\n"
      "v_mov_b32 v59, v26\n"
      "s_mov_b64 exec, -1\n"
      "s_barrier\n"
      ".Lk_wb:\n"
      "s_mov_b32 s60, 0x12000\n"
      "s_mul_i32 s63, s13, 0x480\n"
      "s_add_u32 s60, s60, s63\n"
      "v_and_b32 v1, 3, v0\n"
      "v_lshl_add_u32 v58, v1, 5, s60\n"
      "v_lshrrev_b32 v1, 4, v0\n"
      "v_lshl_add_u32 v1, v1, 3, v58\n"
      "v_lshrrev_b32 v2, 5, v0\n"
      "v_lshl_add_u32 v58, v2, 7, v58\n"
      "v_bfe_u32 v2, v0, 4, 1\n"
      "v_lshl_add_u32 v58, v2, 4, v58\n"
      "v_mov_b32 v2, 0x3f803f80\n"
      "v_mov_b32 v3, 0x3f803f80\n"
      "v_mov_b32 v4, 0x3f803f80\n"
      "v_mov_b32 v5, 0x3f803f80\n"
      "v_mov_b32 v6, 0x3f803f80\n"
      "v_mov_b32 v7, 0x3f803f80\n"
      "v_mov_b32 v8, 0x3f803f80\n"
      "v_mov_b32 v9, 0x3f803f80\n"
      "v_mov_b32 v10, 0x3f803f80\n"
      "v_mov_b32 v11, 0x3f803f80\n"
      "v_mov_b32 v12, 0x3f803f80\n"
      "v_mov_b32 v13, 0x3f803f80\n"
      "v_mov_b32 v14, 0x3f803f80\n"
      "v_mov_b32 v15, 0x3f803f80\n"
      "v_mov_b32 v16, 0x3f803f80\n"
      "v_mov_b32 v17, 0x3f803f80\n"
      "v_mov_b32 v18, 0x3f803f80\n"
      "v_mov_b32 v19, 0x3f803f80\n"
      "v_lshl_add_u32 v56, v0, 3, s92\n"
      "s_add_u32 s60, s92, 2048\n"
      "v_lshrrev_b32 v20, 2, v0\n"
      "v_lshl_add_u32 v57, v20, 3, s60\n"
      "ds_read_b64_tr_b16 v[32:33], v56 offset:9216\n"
      "ds_read_b64_tr_b16 v[34:35], v56 offset:9728\n"
      "ds_read_b64_tr_b16 v[38:39], v57 offset:8192\n"
      "ds_read_b64_tr_b16 v[44:45], v56 offset:0\n"
      "ds_read_b64_tr_b16 v[46:47], v56 offset:512\n"
      "ds_read_b64_tr_b16 v[48:49], v56 offset:1024\n"
      "ds_read_b64_tr_b16 v[50:51], v56 offset:1536\n"
      "ds_read_b64_tr_b16 v[52:53], v57 offset:0\n"
      "ds_read_b64_tr_b16 v[54:55], v57 offset:128\n"
      "ds_read_b64_tr_b16 v[40:41], v57 offset:2304\n"
      "ds_read_b64_tr_b16 v[42:43], v57 offset:2432\n"
      "s_cmp_lt_u32 s12, 36\n"
      "s_cbranch_scc1 .Lk_xd\n"
      "s_sleep 16\n"
      ".Lk_xd:\n"
      "s_mov_b64 exec, s[64:65]\n"
      "buffer_load_dwordx4 v[2:5], v58, s[24:27], s32 offen offset:0 sc1\n"
      "buffer_load_dwordx4 v[6:9], v58, s[24:27], s32 offen offset:256 sc1\n"
      "buffer_load_dwordx4 v[10:13], v58, s[24:27], s32 offen offset:512 sc1\n"
      "buffer_load_dwordx4 v[14:17], v58, s[24:27], s32 offen offset:768 sc1\n"
      "buffer_load_dwordx2 v[18:19], v1, s[24:27], s32 offen offset:1024 sc1\n"
      "s_waitcnt vmcnt(4)\n"
      "v_max_u32 v20, v2, v3\n"
      "v_max3_u32 v20, v20, v4, v5\n"
      "s_waitcnt vmcnt(3)\n"
      "v_max3_u32 v20, v20, v6, v7\n"
      "v_max3_u32 v20, v20, v8, v9\n"
      "s_waitcnt vmcnt(2)\n"
      "v_max3_u32 v20, v20, v10, v11\n"
      "v_max3_u32 v20, v20, v12, v13\n"
      "s_waitcnt vmcnt(1)\n"
      "v_max3_u32 v20, v20, v14, v15\n"
      "v_max3_u32 v20, v20, v16, v17\n"
      "s_waitcnt vmcnt(0)\n"
      "v_max3_u32 v20, v20, v18, v19\n"
      "v_cmp_eq_u32 vcc, -1, v20\n"
      "s_cbranch_vccz .Lk_xk\n"
      "s_waitcnt vmcnt(0) lgkmcnt(0)\n"
      "s_mov_b32 s68, 0x8000\n"
      ".Lk_xh:\n"
      "s_sleep 1\n"
      "buffer_load_dwordx4 v[2:5], v58, s[24:27], s32 offen offset:0 sc1\n"
      "buffer_load_dwordx4 v[6:9], v58, s[24:27], s32 offen offset:256 sc1\n"
      "buffer_load_dwordx4 v[10:13], v58, s[24:27], s32 offen offset:512 sc1\n"
      "buffer_load_dwordx4 v[14:17], v58, s[24:27], s32 offen offset:768 sc1\n"
      "buffer_load_dwordx2 v[18:19], v1, s[24:27], s32 offen offset:1024 sc1\n"
      "s_waitcnt vmcnt(4)\n"
      "v_max_u32 v20, v2, v3\n"
      "v_max3_u32 v20, v20, v4, v5\n"
      "s_waitcnt vmcnt(3)\n"
      "v_max3_u32 v20, v20, v6, v7\n"
      "v_max3_u32 v20, v20, v8, v9\n"
      "s_waitcnt vmcnt(2)\n"
      "v_max3_u32 v20, v20, v10, v11\n"
      "v_max3_u32 v20, v20, v12, v13\n"
      "s_waitcnt vmcnt(1)\n"
      "v_max3_u32 v20, v20, v14, v15\n"
      "v_max3_u32 v20, v20, v16, v17\n"
      "s_waitcnt vmcnt(0)\n"
      "v_max3_u32 v20, v20, v18, v19\n"
      "v_cmp_eq_u32 vcc, -1, v20\n"
      "s_cbranch_vccz .Lk_xk\n"
      "s_sub_u32 s68, s68, 1\n"
      "s_nop 1\n"
      "s_cbranch_scc0 .Lk_xh\n"
      ".Lk_xk:\n"
      "s_mov_b64 exec, -1\n"
      "s_mov_b32 s33, s32\n"
      "s_add_u32 s32, s32, 0x1200\n"
      "s_waitcnt lgkmcnt(8)\n"
      "v_mov_b32_dpp v38, v18 row_shr:4 row_mask:0xf bank_mask:0x6\n"
      "v_mov_b32_dpp v39, v19 row_shr:4 row_mask:0xf bank_mask:0x6\n"
      "v_mfma_f32_16x16x16_bf16 v[20:23], v[32:33], v[18:19], 0\n"
      "v_mfma_f32_16x16x16_bf16 v[24:27], v[34:35], v[18:19], 0\n"
      "ds_read_b64_tr_b16 v[32:33], v56 offset:2304\n"
      "ds_read_b64_tr_b16 v[34:35], v56 offset:2816\n"
      "v_mfma_f32_16x16x16_bf16 v[28:31], v[38:39], v[18:19], 0\n"
      "ds_read_b64_tr_b16 v[36:37], v56 offset:3328\n"
      "ds_read_b64_tr_b16 v[38:39], v56 offset:3840\n"
      "s_waitcnt lgkmcnt(7)\n"
      "v_mov_b32_dpp v52, v2 row_shr:4 row_mask:0xf bank_mask:0x6\n"
      "v_mov_b32_dpp v53, v3 row_shr:4 row_mask:0xf bank_mask:0x6\n"
      "s_waitcnt lgkmcnt(6)\n"
      "v_mov_b32_dpp v54, v4 row_shr:4 row_mask:0xf bank_mask:0x6\n"
      "v_mov_b32_dpp v55, v5 row_shr:4 row_mask:0xf bank_mask:0x6\n"
      "v_mfma_f32_16x16x32_bf16 v[20:23], v[44:47], v[2:5], v[20:23]\n"
      "ds_read_b64_tr_b16 v[44:45], v56 offset:4608\n"
      "ds_read_b64_tr_b16 v[46:47], v56 offset:5120\n"
      "v_mfma_f32_16x16x32_bf16 v[24:27], v[48:51], v[2:5], v[24:27]\n"
      "ds_read_b64_tr_b16 v[48:49], v56 offset:5632\n"
      "ds_read_b64_tr_b16 v[50:51], v56 offset:6144\n"
      "v_mfma_f32_16x16x32_bf16 v[28:31], v[52:55], v[2:5], v[28:31]\n"
      "ds_read_b64_tr_b16 v[52:53], v57 offset:4608\n"
      "ds_read_b64_tr_b16 v[54:55], v57 offset:4736\n"
      "s_mov_b32 s60, 0x18000\n"
      "s_mul_i32 s63, s13, 0x100\n"
      "s_add_u32 s60, s60, s63\n"
      "v_lshrrev_b32 v1, 4, v0\n"
      "v_lshl_add_u32 v58, v1, 6, s60\n"
      "v_and_b32 v1, 3, v0\n"
      "v_lshl_add_u32 v58, v1, 4, v58\n"
      "s_waitcnt lgkmcnt(8)\n"
      "v_mfma_f32_16x16x32_bf16 v[20:23], v[32:35], v[6:9], v[20:23]\n"
      "ds_read_b64_tr_b16 v[32:33], v56 offset:6912\n"
      "ds_read_b64_tr_b16 v[34:35], v56 offset:7424\n"
      "s_waitcnt lgkmcnt(8)\n"
      "v_mfma_f32_16x16x32_bf16 v[24:27], v[36:39], v[6:9], v[24:27]\n"
      "ds_read_b64_tr_b16 v[36:37], v56 offset:7936\n"
      "ds_read_b64_tr_b16 v[38:39], v56 offset:8448\n"
      "v_mov_b32_dpp v40, v6 row_shr:4 row_mask:0xf bank_mask:0x6\n"
      "v_mov_b32_dpp v41, v7 row_shr:4 row_mask:0xf bank_mask:0x6\n"
      "v_mov_b32_dpp v42, v8 row_shr:4 row_mask:0xf bank_mask:0x6\n"
      "v_mov_b32_dpp v43, v9 row_shr:4 row_mask:0xf bank_mask:0x6\n"
      "s_nop 1\n"
      "v_mfma_f32_16x16x32_bf16 v[28:31], v[40:43], v[6:9], v[28:31]\n"
      "ds_read_b64_tr_b16 v[40:41], v57 offset:6912\n"
      "ds_read_b64_tr_b16 v[42:43], v57 offset:7040\n"
      "s_waitcnt lgkmcnt(10)\n"
      "v_mfma_f32_16x16x32_bf16 v[20:23], v[44:47], v[10:13], v[20:23]\n"
      "s_waitcnt lgkmcnt(8)\n"
      "v_mfma_f32_16x16x32_bf16 v[24:27], v[48:51], v[10:13], v[24:27]\n"
      "s_waitcnt lgkmcnt(7)\n"
      "v_mov_b32_dpp v52, v10 row_shr:4 row_mask:0xf bank_mask:0x6\n"
      "v_mov_b32_dpp v53, v11 row_shr:4 row_mask:0xf bank_mask:0x6\n"
      "s_waitcnt lgkmcnt(6)\n"
      "v_mov_b32_dpp v54, v12 row_shr:4 row_mask:0xf bank_mask:0x6\n"
      "v_mov_b32_dpp v55, v13 row_shr:4 row_mask:0xf bank_mask:0x6\n"
      "s_nop 1\n"
      "v_mfma_f32_16x16x32_bf16 v[28:31], v[52:55], v[10:13], v[28:31]\n"
      "s_waitcnt lgkmcnt(4)\n"
      "v_mfma_f32_16x16x32_bf16 v[20:23], v[32:35], v[14:17], v[20:23]\n"
      "s_waitcnt lgkmcnt(2)\n"
      "v_mfma_f32_16x16x32_bf16 v[24:27], v[36:39], v[14:17], v[24:27]\n"
      "s_waitcnt lgkmcnt(1)\n"
      "v_mov_b32_dpp v40, v14 row_shr:4 row_mask:0xf bank_mask:0x6\n"
      "v_mov_b32_dpp v41, v15 row_shr:4 row_mask:0xf bank_mask:0x6\n"
      "s_waitcnt lgkmcnt(0)\n"
      "v_mov_b32_dpp v42, v16 row_shr:4 row_mask:0xf bank_mask:0x6\n"
      "v_mov_b32_dpp v43, v17 row_shr:4 row_mask:0xf bank_mask:0x6\n"
      "s_nop 1\n"
      "v_mfma_f32_16x16x32_bf16 v[28:31], v[40:43], v[14:17], v[28:31]\n"
      "s_mov_b64 exec, s[64:65]\n"
      "ds_write_b128 v58, v[20:23] offset:0\n"
      "ds_write_b128 v58, v[24:27] offset:1024\n"
      "s_nop 4\n"
      "ds_write_b128 v58, v[28:31] offset:2048\n"
      "s_mov_b64 exec, -1\n"
      "v_lshl_add_u32 v1, v0, 3, s93\n"
      "s_add_u32 s60, s93, 4608\n"
      "v_bfe_u32 v3, v0, 2, 1\n"
      "v_lshl_add_u32 v2, v3, 5, s60\n"
      "v_and_b32 v3, 3, v0\n"
      "v_lshl_add_u32 v2, v3, 3, v2\n"
      "ds_read_b64_tr_b16 v[48:49], v1 offset:0\n"
      "ds_read_b64_tr_b16 v[50:51], v2 offset:0\n"
      "ds_read_b64_tr_b16 v[52:53], v1 offset:2048\n"
      "ds_read_b64_tr_b16 v[54:55], v2 offset:256\n"
      "v_mov_b32 v32, 0\n"
      "v_mov_b32 v33, 0\n"
      "v_mov_b32 v34, 0\n"
      "v_mov_b32 v35, 0\n"
      "v_mov_b32 v36, 0\n"
      "v_mov_b32 v37, 0\n"
      "v_mov_b32 v38, 0\n"
      "v_mov_b32 v39, 0\n"
      "v_mov_b32 v40, 0\n"
      "v_mov_b32 v41, 0\n"
      "v_mov_b32 v42, 0\n"
      "v_mov_b32 v43, 0\n"
      "v_mov_b32 v44, 0\n"
      "v_mov_b32 v45, 0\n"
      "v_mov_b32 v46, 0\n"
      "v_mov_b32 v47, 0\n"
      "s_mov_b32 s60, 0x18000\n"
      "v_and_b32 v3, 3, v0\n"
      "v_lshl_add_u32 v56, v3, 4, s60\n"
      "v_bfe_u32 v3, v0, 2, 2\n"
      "v_lshl_add_u32 v56, v3, 2, v56\n"
      "v_add_u32 v57, 0x800, v56\n"
      "v_lshrrev_b32 v3, 4, v0\n"
      "v_lshl_add_u32 v56, v3, 6, v56\n"
      "s_mov_b32 s60, 0x18840\n"
      "v_and_b32 v3, 3, v0\n"
      "v_mul_u32_u24 v3, 0x14, v3\n"
      "v_add_u32 v58, s60, v3\n"
      "s_waitcnt lgkmcnt(0)\n"
      "s_barrier\n"
      "ds_read2_b32 v[12:13], v57 offset0:0 offset1:32\n"
      "ds_read2_b32 v[14:15], v57 offset0:64 offset1:96\n"
      "ds_read2_b32 v[16:17], v57 offset0:128 offset1:160\n"
      "ds_read2_b32 v[18:19], v57 offset0:192 offset1:224\n"
      "ds_read2st64_b32 v[20:21], v58 offset0:0 offset1:1\n"
      "ds_read2st64_b32 v[22:23], v58 offset0:2 offset1:3\n"
      "ds_read2st64_b32 v[4:5], v56 offset0:0 offset1:1\n"
      "ds_read2st64_b32 v[6:7], v56 offset0:2 offset1:3\n"
      "ds_read2st64_b32 v[8:9], v56 offset0:4 offset1:5\n"
      "ds_read2st64_b32 v[10:11], v56 offset0:6 offset1:7\n"
      "ds_read_b64_tr_b16 v[24:25], v1 offset:4096\n"
      "ds_read_b64_tr_b16 v[26:27], v2 offset:512\n"
      "ds_read_b64_tr_b16 v[28:29], v1 offset:512\n"
      "ds_read_b64_tr_b16 v[30:31], v2 offset:64\n"
      "s_waitcnt lgkmcnt(12)\n"
      "v_pk_add_f32 v[12:13], v[12:13], v[14:15]\n"
      "s_waitcnt lgkmcnt(10)\n"
      "v_pk_add_f32 v[16:17], v[16:17], v[18:19]\n"
      "s_waitcnt lgkmcnt(8)\n"
      "v_pk_add_f32 v[20:21], v[20:21], v[22:23]\n"
      "v_pk_add_f32 v[12:13], v[12:13], v[16:17]\n"
      "v_add_f32 v20, v20, v21\n"
      "v_mul_f32 v56, 0xbae38e39, v13\n"
      "v_mul_f32 v20, 0x3ae38e39, v20\n"
      "v_fma_f32 v20, -v56, v56, v20\n"
      "v_max_f32 v20, 0, v20\n"
      "v_add_f32 v20, 0x3727c5ac, v20\n"
      "v_rsq_f32 v57, v20\n"
      "s_waitcnt lgkmcnt(7)\n"
      "v_add_f32 v4, v4, v5\n"
      "s_waitcnt lgkmcnt(6)\n"
      "v_add_f32 v6, v6, v7\n"
      "v_add_f32 v4, v4, v6\n"
      "s_waitcnt lgkmcnt(5)\n"
      "v_add_f32 v8, v8, v9\n"
      "s_waitcnt lgkmcnt(4)\n"
      "v_add_f32 v10, v10, v11\n"
      "v_add_f32 v8, v8, v10\n"
      "v_fmac_f32_dpp v4, v59, v56 quad_perm:[0,0,0,0] row_mask:0xf bank_mask:0xf\n"
      "v_fmac_f32_dpp v8, v59, v56 quad_perm:[1,1,1,1] row_mask:0xf bank_mask:0xf\n"
      "v_fmac_f32_dpp v12, v59, v56 quad_perm:[3,3,3,3] row_mask:0xf bank_mask:0xf\n"
      "v_mul_f32 v58, v8, v57\n"
      "v_mul_f32 v56, v4, v57\n"
      "v_mul_f32 v57, v12, v57\n"
      "ds_read_b64_tr_b16 v[4:5], v1 offset:1024\n"
      "ds_read_b64_tr_b16 v[6:7], v2 offset:128\n"
      "ds_read_b64_tr_b16 v[8:9], v1 offset:1536\n"
      "ds_read_b64_tr_b16 v[10:11], v2 offset:192\n"
      "ds_read_b64_tr_b16 v[12:13], v1 offset:2560\n"
      "ds_read_b64_tr_b16 v[14:15], v2 offset:320\n"
      "ds_read_b64_tr_b16 v[16:17], v1 offset:3072\n"
      "ds_read_b64_tr_b16 v[18:19], v2 offset:384\n"
      "v_mul_f32 v20, 0xbfb8aa3b, v56\n"
      "v_mul_f32 v21, 0xbfb8aa3b, v57\n"
      "v_mov_b32_dpp v3, v57 row_shl:8 row_mask:0xf bank_mask:0xf\n"
      "v_exp_f32 v20, v20\n"
      "v_exp_f32 v21, v21\n"
      "v_mul_f32 v56, v56, v58\n"
      "v_mul_f32 v57, v57, v3\n"
      "v_add_f32 v20, 1.0, v20\n"
      "v_add_f32 v21, 1.0, v21\n"
      "v_rcp_f32 v20, v20\n"
      "v_rcp_f32 v21, v21\n"
      "v_mul_f32 v56, v56, v20\n"
      "v_mul_f32 v57, v57, v21\n"
      "ds_read_b64_tr_b16 v[20:21], v1 offset:3584\n"
      "ds_read_b64_tr_b16 v[22:23], v2 offset:448\n"
      "v_mov_b32_dpp v58, v56 row_shl:4 row_mask:0xf bank_mask:0xf\n"
      "v_mov_b32_dpp v3, v57 row_shl:4 row_mask:0xf bank_mask:0xf\n"
      "v_cvt_pk_bf16_f32 v56, v56, v58\n"
      "v_cvt_pk_bf16_f32 v57, v57, v3\n"
      "s_mov_b32 s60, 0xa0000\n"
      "s_mul_i32 s63, s13, 0x48000\n"
      "s_add_u32 s60, s60, s63\n"
      "s_mul_i32 s63, s12, 0x80\n"
      "s_add_u32 s60, s60, s63\n"
      "v_bfe_u32 v58, v0, 2, 2\n"
      "v_lshl_add_u32 v3, v58, 15, s60\n"
      "v_and_b32 v58, 3, v0\n"
      "v_lshl_add_u32 v3, v58, 5, v3\n"
      "v_lshrrev_b32 v58, 4, v0\n"
      "v_lshl_add_u32 v3, v58, 3, v3\n"
      "v_mov_b32_dpp v32, v56 quad_perm:[0,1,2,3] row_mask:0xf bank_mask:0x1\n"
      "v_mov_b32_dpp v33, v56 row_shl:8 row_mask:0xf bank_mask:0x1\n"
      "v_mov_b32_dpp v34, v57 quad_perm:[0,1,2,3] row_mask:0x1 bank_mask:0x1\n"
      "s_waitcnt lgkmcnt(12)\n"
      "s_nop 0\n"
      "v_mfma_f32_16x16x32_bf16 v[24:27], v[24:27], v[32:35], 0\n"
      "v_mov_b32_dpp v36, v56 row_shr:4 row_mask:0xf bank_mask:0x2\n"
      "v_mov_b32_dpp v37, v56 row_shl:4 row_mask:0xf bank_mask:0x2\n"
      "v_mov_b32_dpp v38, v57 row_shr:4 row_mask:0x1 bank_mask:0x2\n"
      "v_mfma_f32_16x16x32_bf16 v[48:51], v[48:51], v[32:35], 0\n"
      "v_mov_b32_dpp v40, v56 row_shr:8 row_mask:0xf bank_mask:0x4\n"
      "v_mov_b32_dpp v41, v56 quad_perm:[0,1,2,3] row_mask:0xf bank_mask:0x4\n"
      "v_mov_b32_dpp v42, v57 row_shr:8 row_mask:0x1 bank_mask:0x4\n"
      "s_waitcnt lgkmcnt(10)\n"
      "v_mfma_f32_16x16x32_bf16 v[48:51], v[28:31], v[36:39], v[48:51]\n"
      "v_mov_b32_dpp v44, v56 row_shr:12 row_mask:0xf bank_mask:0x8\n"
      "v_mov_b32_dpp v45, v56 row_shr:4 row_mask:0xf bank_mask:0x8\n"
      "v_mov_b32_dpp v46, v57 row_shr:12 row_mask:0x1 bank_mask:0x8\n"
      "s_waitcnt lgkmcnt(8)\n"
      "v_mfma_f32_16x16x32_bf16 v[48:51], v[4:7], v[40:43], v[48:51]\n"
      "s_waitcnt lgkmcnt(6)\n"
      "v_mfma_f32_16x16x32_bf16 v[48:51], v[8:11], v[44:47], v[48:51]\n"
      "v_mfma_f32_16x16x32_bf16 v[52:55], v[52:55], v[32:35], 0\n"
      "s_waitcnt lgkmcnt(4)\n"
      "v_mfma_f32_16x16x32_bf16 v[52:55], v[12:15], v[36:39], v[52:55]\n"
      "v_cvt_pk_bf16_f32 v24, v24, v25\n"
      "v_cvt_pk_bf16_f32 v25, v26, v27\n"
      "s_mov_b64 exec, s[64:65]\n"
      "buffer_store_dwordx2 v[24:25], v3, s[24:27], s31 offen sc1\n"
      "s_mov_b64 exec, -1\n"
      "s_waitcnt lgkmcnt(2)\n"
      "v_mfma_f32_16x16x32_bf16 v[52:55], v[16:19], v[40:43], v[52:55]\n"
      "s_waitcnt lgkmcnt(0)\n"
      "v_mfma_f32_16x16x32_bf16 v[52:55], v[20:23], v[44:47], v[52:55]\n"
      "v_cvt_pk_bf16_f32 v48, v48, v49\n"
      "v_cvt_pk_bf16_f32 v49, v50, v51\n"
      "buffer_store_dwordx2 v[48:49], v3, s[24:27], s29 offen sc1\n"
      "s_nop 4\n"
      "v_cvt_pk_bf16_f32 v52, v52, v53\n"
      "v_cvt_pk_bf16_f32 v53, v54, v55\n"
      "buffer_store_dwordx2 v[52:53], v3, s[24:27], s30 offen sc1\n"
      "s_barrier\n"
      "s_cmp_ge_u32 s12, 36\n"
      "s_cbranch_scc1 .Lk_sx\n"
      "s_mov_b32 s60, 0xa0000\n"
      "s_mul_i32 s63, s12, 0x8000\n"
      "s_add_u32 s60, s60, s63\n"
      "s_mul_i32 s63, s13, 0x2000\n"
      "s_add_u32 s60, s60, s63\n"
      "v_lshl_add_u32 v2, v0, 4, s60\n"
      "v_add_u32 v4, 0x1000, v2\n"
      "v_and_b32 v1, 15, v0\n"
      "v_mov_b32 v10, 0\n"
      "v_cmp_eq_u32 vcc, 0, v1\n"
      "v_mov_b32 v5, 0x3f80\n"
      "s_nop 0\n"
      "v_cndmask_b32 v10, v10, v5, vcc\n"
      "v_cmp_eq_u32 vcc, 1, v1\n"
      "v_mov_b32 v5, 1.0\n"
      "s_nop 0\n"
      "v_cndmask_b32 v10, v10, v5, vcc\n"
      "v_mov_b32 v11, 0\n"
      "v_cmp_eq_u32 vcc, 2, v1\n"
      "v_mov_b32 v5, 0x3f80\n"
      "s_nop 0\n"
      "v_cndmask_b32 v11, v11, v5, vcc\n"
      "v_cmp_eq_u32 vcc, 3, v1\n"
      "v_mov_b32 v5, 1.0\n"
      "s_nop 0\n"
      "v_cndmask_b32 v11, v11, v5, vcc\n"
      "v_mov_b32 v12, 0\n"
      "v_cmp_eq_u32 vcc, 4, v1\n"
      "v_mov_b32 v5, 0x3f80\n"
      "s_nop 0\n"
      "v_cndmask_b32 v12, v12, v5, vcc\n"
      "v_cmp_eq_u32 vcc, 5, v1\n"
      "v_mov_b32 v5, 1.0\n"
      "s_nop 0\n"
      "v_cndmask_b32 v12, v12, v5, vcc\n"
      "v_mov_b32 v13, 0\n"
      "v_cmp_eq_u32 vcc, 6, v1\n"
      "v_mov_b32 v5, 0x3f80\n"
      "s_nop 0\n"
      "v_cndmask_b32 v13, v13, v5, vcc\n"
      "v_cmp_eq_u32 vcc, 7, v1\n"
      "v_mov_b32 v5, 1.0\n"
      "s_nop 0\n"
      "v_cndmask_b32 v13, v13, v5, vcc\n"
      "s_mov_b32 s60, 0x12000\n"
      "s_mul_i32 s63, s12, 0x80\n"
      "s_add_u32 s60, s60, s63\n"
      "v_and_b32 v1, 7, v0\n"
      "v_lshl_add_u32 v5, v1, 4, s60\n"
      "v_bfe_u32 v1, v0, 4, 1\n"
      "v_lshl_add_u32 v5, v1, 3, v5\n"
      "s_mov_b32 s60, 0x0\n"
      "s_mul_i32 s63, s12, 0x20\n"
      "s_add_u32 s60, s60, s63\n"
      "v_bfe_u32 v1, v0, 1, 2\n"
      "v_mul_u32_u24 v1, 0x480, v1\n"
      "v_add_u32 v6, s60, v1\n"
      "v_and_b32 v1, 1, v0\n"
      "v_lshl_add_u32 v6, v1, 4, v6\n"
      "v_bfe_u32 v1, v0, 4, 1\n"
      "v_lshl_add_u32 v6, v1, 3, v6\n"
      "s_mov_b32 s60, 0x17c00\n"
      "s_mul_i32 s63, s13, 0x400\n"
      "s_add_u32 s60, s60, s63\n"
      "v_and_b32 v1, 7, v0\n"
      "v_lshl_add_u32 v7, v1, 4, s60\n"
      "v_bfe_u32 v1, v0, 4, 1\n"
      "v_lshl_add_u32 v7, v1, 7, v7\n"
      "s_mov_b64 exec, 0xff00ff\n"
      "buffer_load_dwordx2 v[18:19], v5, s[24:27], s33 offen sc1\n"
      "s_mov_b64 exec, -1\n"
      "s_sleep 4\n"
      "buffer_load_dwordx4 v[26:29], v2, s[24:27], s29 offen offset:0 sc1\n"
      "buffer_load_dwordx4 v[30:33], v2, s[24:27], s29 offen offset:1024 sc1\n"
      "buffer_load_dwordx4 v[34:37], v2, s[24:27], s29 offen offset:2048 sc1\n"
      "buffer_load_dwordx4 v[38:41], v2, s[24:27], s29 offen offset:3072 sc1\n"
      "buffer_load_dwordx4 v[42:45], v4, s[24:27], s29 offen offset:0 sc1\n"
      "buffer_load_dwordx4 v[46:49], v4, s[24:27], s29 offen offset:1024 sc1\n"
      "buffer_load_dwordx4 v[50:53], v4, s[24:27], s29 offen offset:2048 sc1\n"
      "buffer_load_dwordx4 v[54:57], v4, s[24:27], s29 offen offset:3072 sc1\n"
      "s_waitcnt vmcnt(7)\n"
      "v_max_u32 v1, v26, v27\n"
      "v_max3_u32 v1, v1, v28, v29\n"
      "s_waitcnt vmcnt(6)\n"
      "v_max3_u32 v1, v1, v30, v31\n"
      "v_max3_u32 v1, v1, v32, v33\n"
      "s_waitcnt vmcnt(5)\n"
      "v_max3_u32 v1, v1, v34, v35\n"
      "v_max3_u32 v1, v1, v36, v37\n"
      "s_waitcnt vmcnt(4)\n"
      "v_max3_u32 v1, v1, v38, v39\n"
      "v_max3_u32 v1, v1, v40, v41\n"
      "s_waitcnt vmcnt(3)\n"
      "v_max3_u32 v1, v1, v42, v43\n"
      "v_max3_u32 v1, v1, v44, v45\n"
      "s_waitcnt vmcnt(2)\n"
      "v_max3_u32 v1, v1, v46, v47\n"
      "v_max3_u32 v1, v1, v48, v49\n"
      "s_waitcnt vmcnt(1)\n"
      "v_max3_u32 v1, v1, v50, v51\n"
      "v_max3_u32 v1, v1, v52, v53\n"
      "s_waitcnt vmcnt(0)\n"
      "v_max3_u32 v1, v1, v54, v55\n"
      "v_max3_u32 v1, v1, v56, v57\n"
      "v_cmp_eq_u32 vcc, -1, v1\n"
      "s_cbranch_vccz .Lk_sk\n"
      "s_waitcnt vmcnt(0) lgkmcnt(0)\n"
      "s_mov_b32 s68, 0x8000\n"
      ".Lk_sh:\n"
      "s_sleep 1\n"
      "buffer_load_dwordx4 v[26:29], v2, s[24:27], s29 offen offset:0 sc1\n"
      "buffer_load_dwordx4 v[30:33], v2, s[24:27], s29 offen offset:1024 sc1\n"
      "buffer_load_dwordx4 v[34:37], v2, s[24:27], s29 offen offset:2048 sc1\n"
      "buffer_load_dwordx4 v[38:41], v2, s[24:27], s29 offen offset:3072 sc1\n"
      "buffer_load_dwordx4 v[42:45], v4, s[24:27], s29 offen offset:0 sc1\n"
      "buffer_load_dwordx4 v[46:49], v4, s[24:27], s29 offen offset:1024 sc1\n"
      "buffer_load_dwordx4 v[50:53], v4, s[24:27], s29 offen offset:2048 sc1\n"
      "buffer_load_dwordx4 v[54:57], v4, s[24:27], s29 offen offset:3072 sc1\n"
      "s_waitcnt vmcnt(7)\n"
      "v_max_u32 v1, v26, v27\n"
      "v_max3_u32 v1, v1, v28, v29\n"
      "s_waitcnt vmcnt(6)\n"
      "v_max3_u32 v1, v1, v30, v31\n"
      "v_max3_u32 v1, v1, v32, v33\n"
      "s_waitcnt vmcnt(5)\n"
      "v_max3_u32 v1, v1, v34, v35\n"
      "v_max3_u32 v1, v1, v36, v37\n"
      "s_waitcnt vmcnt(4)\n"
      "v_max3_u32 v1, v1, v38, v39\n"
      "v_max3_u32 v1, v1, v40, v41\n"
      "s_waitcnt vmcnt(3)\n"
      "v_max3_u32 v1, v1, v42, v43\n"
      "v_max3_u32 v1, v1, v44, v45\n"
      "s_waitcnt vmcnt(2)\n"
      "v_max3_u32 v1, v1, v46, v47\n"
      "v_max3_u32 v1, v1, v48, v49\n"
      "s_waitcnt vmcnt(1)\n"
      "v_max3_u32 v1, v1, v50, v51\n"
      "v_max3_u32 v1, v1, v52, v53\n"
      "s_waitcnt vmcnt(0)\n"
      "v_max3_u32 v1, v1, v54, v55\n"
      "v_max3_u32 v1, v1, v56, v57\n"
      "v_cmp_eq_u32 vcc, -1, v1\n"
      "s_cbranch_vccz .Lk_sk\n"
      "s_sub_u32 s68, s68, 1\n"
      "s_nop 1\n"
      "s_cbranch_scc0 .Lk_sh\n"
      ".Lk_sk:\n"
      "v_mfma_f32_16x16x32_bf16 v[14:17], v[10:13], v[26:29], 0\n"
      "v_mfma_f32_16x16x32_bf16 v[14:17], v[10:13], v[30:33], v[14:17]\n"
      "v_mfma_f32_16x16x32_bf16 v[14:17], v[10:13], v[34:37], v[14:17]\n"
      "v_mfma_f32_16x16x32_bf16 v[14:17], v[10:13], v[38:41], v[14:17]\n"
      "v_mfma_f32_16x16x32_bf16 v[14:17], v[10:13], v[42:45], v[14:17]\n"
      "v_mfma_f32_16x16x32_bf16 v[14:17], v[10:13], v[46:49], v[14:17]\n"
      "v_mfma_f32_16x16x32_bf16 v[14:17], v[10:13], v[50:53], v[14:17]\n"
      "v_mfma_f32_16x16x32_bf16 v[14:17], v[10:13], v[54:57], v[14:17]\n"
      "s_nop 7\n"
      "v_add_f32_dpp v14, v14, v14 row_ror:8 row_mask:0xf bank_mask:0xf\n"
      "v_add_f32_dpp v15, v15, v15 row_ror:8 row_mask:0xf bank_mask:0xf\n"
      "v_add_f32_dpp v16, v16, v16 row_ror:8 row_mask:0xf bank_mask:0xf\n"
      "v_add_f32_dpp v17, v17, v17 row_ror:8 row_mask:0xf bank_mask:0xf\n"
      "s_mov_b64 exec, 0xff00ff\n"
      "s_cmp_eq_u32 s13, 0\n"
      "s_cbranch_scc1 .Lk_sz\n"
      "ds_write_b128 v7, v[14:17]\n"
      "s_waitcnt lgkmcnt(0)\n"
      "s_barrier\n"
      "s_barrier\n"
      "s_branch .Lk_se\n"
      ".Lk_sz:\n"
      "s_barrier\n"
      "ds_read_b128 v[26:29], v7 offset:1024\n"
      "ds_read_b128 v[30:33], v7 offset:2048\n"
      "ds_read_b128 v[34:37], v7 offset:3072\n"
      "v_lshlrev_b32 v20, 16, v18\n"
      "v_and_b32 v21, 0xffff0000, v18\n"
      "v_lshlrev_b32 v22, 16, v19\n"
      "v_and_b32 v23, 0xffff0000, v19\n"
      "s_waitcnt lgkmcnt(0)\n"
      "s_barrier\n"
      "v_pk_add_f32 v[14:15], v[14:15], v[26:27]\n"
      "v_pk_add_f32 v[16:17], v[16:17], v[28:29]\n"
      "v_pk_add_f32 v[14:15], v[14:15], v[30:31]\n"
      "v_pk_add_f32 v[16:17], v[16:17], v[32:33]\n"
      "v_pk_add_f32 v[14:15], v[14:15], v[34:35]\n"
      "v_pk_add_f32 v[16:17], v[16:17], v[36:37]\n"
      "v_cvt_pk_bf16_f32 v18, v14, v15\n"
      "v_cvt_pk_bf16_f32 v19, v16, v17\n"
      "v_lshlrev_b32 v14, 16, v18\n"
      "v_and_b32 v15, 0xffff0000, v18\n"
      "v_lshlrev_b32 v16, 16, v19\n"
      "v_and_b32 v17, 0xffff0000, v19\n"
      "v_pk_add_f32 v[14:15], v[14:15], v[20:21]\n"
      "v_pk_add_f32 v[16:17], v[16:17], v[22:23]\n"
      "v_cvt_pk_bf16_f32 v18, v14, v15\n"
      "v_cvt_pk_bf16_f32 v19, v16, v17\n"
      "s_cmp_eq_u32 s15, 0\n"
      "s_cbranch_scc1 .Lk_ss\n"
      "buffer_store_dwordx2 v[18:19], v6, s[52:55], 0 offen\n"
      "s_branch .Lk_se\n"
      ".Lk_ss:\n"
      "buffer_store_dwordx2 v[18:19], v5, s[24:27], s32 offen sc1\n"
      ".Lk_se:\n"
      "s_mov_b64 exec, -1\n"
      ".Lk_sx:\n"
      "s_add_u32 s29, s29, 0x120000\n"
      "s_add_u32 s30, s30, 0x120000\n"
      "s_add_u32 s31, s31, 0x120000\n"
      "s_cmp_eq_u32 s15, 1\n"
      "s_cbranch_scc1 .Lk_end\n"
      "s_add_u32 s28, s28, 0x500000\n"
      "s_mov_b32 s15, 1\n"
      "s_mov_b32 s60, 0x12000\n"
      "s_mul_i32 s63, s13, 0x480\n"
      "s_add_u32 s60, s60, s63\n"
      "v_and_b32 v1, 3, v0\n"
      "v_lshl_add_u32 v58, v1, 5, s60\n"
      "v_lshrrev_b32 v1, 4, v0\n"
      "v_lshl_add_u32 v1, v1, 3, v58\n"
      "v_lshrrev_b32 v2, 5, v0\n"
      "v_lshl_add_u32 v58, v2, 7, v58\n"
      "v_bfe_u32 v2, v0, 4, 1\n"
      "v_lshl_add_u32 v58, v2, 4, v58\n"
      "v_mov_b32 v2, 0x3f803f80\n"
      "v_mov_b32 v3, 0x3f803f80\n"
      "v_mov_b32 v4, 0x3f803f80\n"
      "v_mov_b32 v5, 0x3f803f80\n"
      "v_mov_b32 v6, 0x3f803f80\n"
      "v_mov_b32 v7, 0x3f803f80\n"
      "v_mov_b32 v8, 0x3f803f80\n"
      "v_mov_b32 v9, 0x3f803f80\n"
      "v_mov_b32 v10, 0x3f803f80\n"
      "v_mov_b32 v11, 0x3f803f80\n"
      "v_mov_b32 v12, 0x3f803f80\n"
      "v_mov_b32 v13, 0x3f803f80\n"
      "v_mov_b32 v14, 0x3f803f80\n"
      "v_mov_b32 v15, 0x3f803f80\n"
      "v_mov_b32 v16, 0x3f803f80\n"
      "v_mov_b32 v17, 0x3f803f80\n"
      "v_mov_b32 v18, 0x3f803f80\n"
      "v_mov_b32 v19, 0x3f803f80\n"
      "s_mov_b32 s60, 0x18000\n"
      "s_mul_i32 s63, s13, 0x100\n"
      "s_add_u32 s60, s60, s63\n"
      "v_lshrrev_b32 v20, 4, v0\n"
      "v_lshl_add_u32 v36, v20, 6, s60\n"
      "v_and_b32 v20, 3, v0\n"
      "v_lshl_add_u32 v36, v20, 4, v36\n"
      "s_mov_b32 s60, 0x18000\n"
      "v_and_b32 v20, 3, v0\n"
      "v_lshl_add_u32 v37, v20, 4, s60\n"
      "v_bfe_u32 v20, v0, 2, 2\n"
      "v_lshl_add_u32 v37, v20, 2, v37\n"
      "v_add_u32 v38, 0x800, v37\n"
      "v_lshrrev_b32 v20, 4, v0\n"
      "v_lshl_add_u32 v37, v20, 6, v37\n"
      "s_mov_b32 s60, 0x18840\n"
      "v_and_b32 v20, 3, v0\n"
      "v_mul_u32_u24 v20, 0x14, v20\n"
      "v_add_u32 v39, s60, v20\n"
      "s_mov_b32 s60, 0x2000\n"
      "s_mul_i32 s63, s56, 0x80\n"
      "s_add_u32 s60, s60, s63\n"
      "v_and_b32 v20, 3, v0\n"
      "v_lshl_add_u32 v40, v20, 5, s60\n"
      "v_lshrrev_b32 v20, 4, v0\n"
      "v_lshl_add_u32 v40, v20, 3, v40\n"
      "v_bfe_u32 v20, v0, 3, 1\n"
      "v_lshl_add_u32 v40, v20, 2, v40\n"
      "s_lshr_b32 s61, s13, 1\n"
      "s_mov_b32 s60, 0x2000\n"
      "s_mul_i32 s63, s57, 0x800\n"
      "s_add_u32 s60, s60, s63\n"
      "s_mul_i32 s63, s70, 0x20\n"
      "s_add_u32 s60, s60, s63\n"
      "v_and_b32 v20, 1, v0\n"
      "v_xor_b32 v20, s61, v20\n"
      "v_lshl_add_u32 v41, v20, 10, s60\n"
      "v_lshrrev_b32 v20, 5, v0\n"
      "v_lshl_add_u32 v41, v20, 7, v41\n"
      "v_bfe_u32 v20, v0, 4, 1\n"
      "v_lshl_add_u32 v41, v20, 4, v41\n"
      "s_cmp_lt_u32 s12, 36\n"
      "s_cbranch_scc1 .Lk_yd\n"
      "s_sleep 16\n"
      ".Lk_yd:\n"
      "s_mov_b64 exec, s[64:65]\n"
      "buffer_load_dwordx4 v[2:5], v58, s[24:27], s32 offen offset:0 sc1\n"
      "buffer_load_dwordx4 v[6:9], v58, s[24:27], s32 offen offset:256 sc1\n"
      "buffer_load_dwordx4 v[10:13], v58, s[24:27], s32 offen offset:512 sc1\n"
      "buffer_load_dwordx4 v[14:17], v58, s[24:27], s32 offen offset:768 sc1\n"
      "buffer_load_dwordx2 v[18:19], v1, s[24:27], s32 offen offset:1024 sc1\n"
      "s_waitcnt vmcnt(4)\n"
      "v_max_u32 v20, v2, v3\n"
      "v_max3_u32 v20, v20, v4, v5\n"
      "s_waitcnt vmcnt(3)\n"
      "v_max3_u32 v20, v20, v6, v7\n"
      "v_max3_u32 v20, v20, v8, v9\n"
      "s_waitcnt vmcnt(2)\n"
      "v_max3_u32 v20, v20, v10, v11\n"
      "v_max3_u32 v20, v20, v12, v13\n"
      "s_waitcnt vmcnt(1)\n"
      "v_max3_u32 v20, v20, v14, v15\n"
      "v_max3_u32 v20, v20, v16, v17\n"
      "s_waitcnt vmcnt(0)\n"
      "v_max3_u32 v20, v20, v18, v19\n"
      "v_cmp_eq_u32 vcc, -1, v20\n"
      "s_cbranch_vccz .Lk_yk\n"
      "s_waitcnt vmcnt(0) lgkmcnt(0)\n"
      "s_mov_b32 s68, 0x8000\n"
      ".Lk_yh:\n"
      "s_sleep 1\n"
      "buffer_load_dwordx4 v[2:5], v58, s[24:27], s32 offen offset:0 sc1\n"
      "buffer_load_dwordx4 v[6:9], v58, s[24:27], s32 offen offset:256 sc1\n"
      "buffer_load_dwordx4 v[10:13], v58, s[24:27], s32 offen offset:512 sc1\n"
      "buffer_load_dwordx4 v[14:17], v58, s[24:27], s32 offen offset:768 sc1\n"
      "buffer_load_dwordx2 v[18:19], v1, s[24:27], s32 offen offset:1024 sc1\n"
      "s_waitcnt vmcnt(4)\n"
      "v_max_u32 v20, v2, v3\n"
      "v_max3_u32 v20, v20, v4, v5\n"
      "s_waitcnt vmcnt(3)\n"
      "v_max3_u32 v20, v20, v6, v7\n"
      "v_max3_u32 v20, v20, v8, v9\n"
      "s_waitcnt vmcnt(2)\n"
      "v_max3_u32 v20, v20, v10, v11\n"
      "v_max3_u32 v20, v20, v12, v13\n"
      "s_waitcnt vmcnt(1)\n"
      "v_max3_u32 v20, v20, v14, v15\n"
      "v_max3_u32 v20, v20, v16, v17\n"
      "s_waitcnt vmcnt(0)\n"
      "v_max3_u32 v20, v20, v18, v19\n"
      "v_cmp_eq_u32 vcc, -1, v20\n"
      "s_cbranch_vccz .Lk_yk\n"
      "s_sub_u32 s68, s68, 1\n"
      "s_nop 1\n"
      "s_cbranch_scc0 .Lk_yh\n"
      ".Lk_yk:\n"
      "s_mov_b64 exec, -1\n"
      "s_mov_b32 s33, s32\n"
      "s_add_u32 s32, s32, 0x1200\n"
      "v_mov_b32_dpp v20, v18 row_shr:4 row_mask:0xf bank_mask:0x6\n"
      "v_mov_b32_dpp v21, v19 row_shr:4 row_mask:0xf bank_mask:0x6\n"
      "v_mfma_f32_16x16x16_bf16 v[28:31], v[76:77], v[18:19], 0\n"
      "v_mov_b32_dpp v24, v2 row_shr:4 row_mask:0xf bank_mask:0x6\n"
      "v_mov_b32_dpp v25, v3 row_shr:4 row_mask:0xf bank_mask:0x6\n"
      "v_mov_b32_dpp v26, v4 row_shr:4 row_mask:0xf bank_mask:0x6\n"
      "v_mov_b32_dpp v27, v5 row_shr:4 row_mask:0xf bank_mask:0x6\n"
      "v_mfma_f32_16x16x16_bf16 v[32:35], v[20:21], v[18:19], 0\n"
      "v_mov_b32_dpp v20, v6 row_shr:4 row_mask:0xf bank_mask:0x6\n"
      "v_mov_b32_dpp v21, v7 row_shr:4 row_mask:0xf bank_mask:0x6\n"
      "v_mov_b32_dpp v22, v8 row_shr:4 row_mask:0xf bank_mask:0x6\n"
      "v_mov_b32_dpp v23, v9 row_shr:4 row_mask:0xf bank_mask:0x6\n"
      "v_mfma_f32_16x16x32_bf16 v[28:31], v[60:63], v[2:5], v[28:31]\n"
      "s_nop 0\n"
      "v_mfma_f32_16x16x32_bf16 v[32:35], v[24:27], v[2:5], v[32:35]\n"
      "v_mov_b32_dpp v24, v10 row_shr:4 row_mask:0xf bank_mask:0x6\n"
      "v_mov_b32_dpp v25, v11 row_shr:4 row_mask:0xf bank_mask:0x6\n"
      "v_mov_b32_dpp v26, v12 row_shr:4 row_mask:0xf bank_mask:0x6\n"
      "v_mov_b32_dpp v27, v13 row_shr:4 row_mask:0xf bank_mask:0x6\n"
      "v_mfma_f32_16x16x32_bf16 v[28:31], v[64:67], v[6:9], v[28:31]\n"
      "v_mfma_f32_16x16x32_bf16 v[32:35], v[20:23], v[6:9], v[32:35]\n"
      "v_mov_b32_dpp v20, v14 row_shr:4 row_mask:0xf bank_mask:0x6\n"
      "v_mov_b32_dpp v21, v15 row_shr:4 row_mask:0xf bank_mask:0x6\n"
      "v_mov_b32_dpp v22, v16 row_shr:4 row_mask:0xf bank_mask:0x6\n"
      "v_mov_b32_dpp v23, v17 row_shr:4 row_mask:0xf bank_mask:0x6\n"
      "v_mfma_f32_16x16x32_bf16 v[28:31], v[68:71], v[10:13], v[28:31]\n"
      "v_mfma_f32_16x16x32_bf16 v[32:35], v[24:27], v[10:13], v[32:35]\n"
      "v_mfma_f32_16x16x32_bf16 v[28:31], v[72:75], v[14:17], v[28:31]\n"
      "v_mfma_f32_16x16x32_bf16 v[32:35], v[20:23], v[14:17], v[32:35]\n"
      "s_mov_b64 exec, s[64:65]\n"
      "s_nop 5\n"
      "ds_write_b128 v36, v[28:31]\n"
      "ds_write_b128 v36, v[32:35] offset:2048\n"
      "s_mov_b64 exec, -1\n"
      "s_mov_b32 s62, 0x8000\n"
      "s_waitcnt lgkmcnt(0)\n"
      "s_barrier\n"
      "s_cmp_lg_u32 s13, 0\n"
      "s_cbranch_scc1 .Lk_q2\n"
      "ds_read2_b32 v[6:7], v38 offset0:0 offset1:32\n"
      "ds_read2_b32 v[8:9], v38 offset0:64 offset1:96\n"
      "ds_read2_b32 v[10:11], v38 offset0:128 offset1:160\n"
      "ds_read2_b32 v[12:13], v38 offset0:192 offset1:224\n"
      "ds_read2st64_b32 v[14:15], v39 offset0:0 offset1:1\n"
      "ds_read2st64_b32 v[16:17], v39 offset0:2 offset1:3\n"
      "ds_read2st64_b32 v[2:3], v37 offset0:0 offset1:1\n"
      "ds_read2st64_b32 v[4:5], v37 offset0:2 offset1:3\n"
      "s_waitcnt lgkmcnt(1)\n"
      "v_add_f32 v2, v2, v3\n"
      "s_waitcnt lgkmcnt(0)\n"
      "v_add_f32 v4, v4, v5\n"
      "v_add_f32 v2, v2, v4\n"
      "v_pk_add_f32 v[6:7], v[6:7], v[8:9]\n"
      "v_pk_add_f32 v[10:11], v[10:11], v[12:13]\n"
      "v_pk_add_f32 v[14:15], v[14:15], v[16:17]\n"
      "v_pk_add_f32 v[6:7], v[6:7], v[10:11]\n"
      "v_add_f32 v14, v14, v15\n"
      "v_mul_f32 v18, 0xbae38e39, v7\n"
      "v_mul_f32 v14, 0x3ae38e39, v14\n"
      "v_fma_f32 v14, -v18, v18, v14\n"
      "v_max_f32 v14, 0, v14\n"
      "v_add_f32 v14, 0x3727c5ac, v14\n"
      "v_rsq_f32 v19, v14\n"
      "v_fmac_f32_dpp v2, v59, v18 quad_perm:[2,2,2,2] row_mask:0xf bank_mask:0xf\n"
      "v_mul_f32 v2, v2, v19\n"
      "s_nop 1\n"
      "v_mov_b32_dpp v3, v2 row_shl:4 row_mask:0xf bank_mask:0xf\n"
      "s_mov_b32 exec_lo, 0xf0f0f0f\n"
      "s_mov_b32 exec_hi, 0xf0f0f0f\n"
      "v_cvt_pk_bf16_f32 v2, v2, v3\n"
      "buffer_store_dword v2, v40, s[24:27], s62 offen sc1\n"
      ".Lk_q2:\n"
      "s_mov_b64 exec, -1\n"
      "s_barrier\n"
      "s_and_b32 s61, s13, 1\n"
      "s_lshl_b32 s61, s61, 5\n"
      "v_bfe_u32 v1, v0, 4, 1\n"
      "v_lshlrev_b32 v6, 7, v1\n"
      "v_bfe_u32 v1, v0, 2, 2\n"
      "v_lshl_add_u32 v6, v1, 5, v6\n"
      "v_bfe_u32 v1, v0, 1, 1\n"
      "v_lshl_add_u32 v6, v1, 4, v6\n"
      "v_and_b32 v1, 1, v0\n"
      "v_lshl_add_u32 v6, v1, 3, v6\n"
      "v_xor_b32 v6, s61, v6\n"
      "v_add_u32 v6, s58, v6\n"
      "v_bfe_u32 v1, v0, 2, 2\n"
      "v_lshl_add_u32 v6, v1, 10, v6\n"
      "v_lshrrev_b32 v1, 4, v0\n"
      "v_lshl_add_u32 v6, v1, 8, v6\n"
      "s_mov_b32 s60, 0x18000\n"
      "s_mul_i32 s63, s13, 0x100\n"
      "s_add_u32 s60, s60, s63\n"
      "v_lshl_add_u32 v56, v0, 2, s60\n"
      "v_xor_b32 v2, 0x100, v56\n"
      "v_xor_b32 v3, 0x200, v56\n"
      "v_xor_b32 v4, 0x300, v56\n"
      "v_and_b32 v1, 1, v0\n"
      "v_lshlrev_b32 v57, 1, v1\n"
      "v_bfe_u32 v1, v0, 1, 1\n"
      "v_lshl_add_u32 v57, v1, 0, v57\n"
      "v_xor_b32 v1, s13, v57\n"
      "v_lshlrev_b32 v57, 3, v57\n"
      "v_lshl_add_u32 v57, v1, 5, v57\n"
      "v_add_u32 v57, 0x18c00, v57\n"
      "s_mov_b32 s60, 0x18c00\n"
      "s_mul_i32 s63, s13, 0x20\n"
      "s_add_u32 s60, s60, s63\n"
      "v_mov_b32 v5, s60\n"
      "s_lshr_b32 s61, s13, 1\n"
      "s_and_b32 s62, s13, 1\n"
      "s_mov_b32 s60, 0x20000\n"
      "s_mul_i32 s63, s57, 0x8000\n"
      "s_add_u32 s60, s60, s63\n"
      "s_mul_i32 s63, s61, 0x4000\n"
      "s_add_u32 s60, s60, s63\n"
      "s_mul_i32 s63, s62, 0x800\n"
      "s_add_u32 s60, s60, s63\n"
      "s_mul_i32 s63, s69, 0x80\n"
      "s_add_u32 s60, s60, s63\n"
      "v_lshrrev_b32 v1, 4, v0\n"
      "v_lshl_add_u32 v58, v1, 12, s60\n"
      "v_and_b32 v1, 15, v0\n"
      "v_lshl_add_u32 v58, v1, 3, v58\n"
      "s_mov_b32 s60, 0x20000\n"
      "s_mul_i32 s63, s56, 0x800\n"
      "s_add_u32 s60, s60, s63\n"
      "v_and_b32 v1, 3, v0\n"
      "v_lshl_add_u32 v9, v1, 9, s60\n"
      "v_bfe_u32 v1, v0, 2, 2\n"
      "v_lshl_add_u32 v9, v1, 7, v9\n"
      "v_lshrrev_b32 v1, 4, v0\n"
      "v_lshl_add_u32 v9, v1, 5, v9\n"
      "s_mov_b32 s62, 0x8000\n"
      "s_sleep 4\n"
      "buffer_load_dwordx4 v[10:13], v41, s[24:27], s62 offen offset:0 sc1\n"
      "buffer_load_dwordx4 v[14:17], v41, s[24:27], s62 offen offset:256 sc1\n"
      "buffer_load_dwordx4 v[18:21], v41, s[24:27], s62 offen offset:512 sc1\n"
      "buffer_load_dwordx4 v[22:25], v41, s[24:27], s62 offen offset:768 sc1\n"
      "s_waitcnt vmcnt(3)\n"
      "v_max_u32 v1, v10, v11\n"
      "v_max3_u32 v1, v1, v12, v13\n"
      "s_waitcnt vmcnt(2)\n"
      "v_max3_u32 v1, v1, v14, v15\n"
      "v_max3_u32 v1, v1, v16, v17\n"
      "s_waitcnt vmcnt(1)\n"
      "v_max3_u32 v1, v1, v18, v19\n"
      "v_max3_u32 v1, v1, v20, v21\n"
      "s_waitcnt vmcnt(0)\n"
      "v_max3_u32 v1, v1, v22, v23\n"
      "v_max3_u32 v1, v1, v24, v25\n"
      "v_cmp_eq_u32 vcc, -1, v1\n"
      "s_cbranch_vccz .Lk_qk\n"
      "s_waitcnt vmcnt(0) lgkmcnt(0)\n"
      "s_mov_b32 s68, 0x8000\n"
      ".Lk_qh:\n"
      "s_sleep 1\n"
      "buffer_load_dwordx4 v[10:13], v41, s[24:27], s62 offen offset:0 sc1\n"
      "buffer_load_dwordx4 v[14:17], v41, s[24:27], s62 offen offset:256 sc1\n"
      "buffer_load_dwordx4 v[18:21], v41, s[24:27], s62 offen offset:512 sc1\n"
      "buffer_load_dwordx4 v[22:25], v41, s[24:27], s62 offen offset:768 sc1\n"
      "s_waitcnt vmcnt(3)\n"
      "v_max_u32 v1, v10, v11\n"
      "v_max3_u32 v1, v1, v12, v13\n"
      "s_waitcnt vmcnt(2)\n"
      "v_max3_u32 v1, v1, v14, v15\n"
      "v_max3_u32 v1, v1, v16, v17\n"
      "s_waitcnt vmcnt(1)\n"
      "v_max3_u32 v1, v1, v18, v19\n"
      "v_max3_u32 v1, v1, v20, v21\n"
      "s_waitcnt vmcnt(0)\n"
      "v_max3_u32 v1, v1, v22, v23\n"
      "v_max3_u32 v1, v1, v24, v25\n"
      "v_cmp_eq_u32 vcc, -1, v1\n"
      "s_cbranch_vccz .Lk_qk\n"
      "s_sub_u32 s68, s68, 1\n"
      "s_nop 1\n"
      "s_cbranch_scc0 .Lk_qh\n"
      ".Lk_qk:\n"
      "v_mfma_f32_16x16x32_bf16 v[26:29], a[0:3], v[10:13], 0\n"
      "v_mfma_f32_16x16x32_bf16 v[26:29], a[4:7], v[14:17], v[26:29]\n"
      "v_mfma_f32_16x16x32_bf16 v[26:29], a[8:11], v[18:21], v[26:29]\n"
      "v_mfma_f32_16x16x32_bf16 v[26:29], a[12:15], v[22:25], v[26:29]\n"
      "v_mfma_f32_16x16x32_bf16 v[30:33], a[16:19], v[10:13], 0\n"
      "v_mfma_f32_16x16x32_bf16 v[30:33], a[20:23], v[14:17], v[30:33]\n"
      "v_mfma_f32_16x16x32_bf16 v[30:33], a[24:27], v[18:21], v[30:33]\n"
      "v_mfma_f32_16x16x32_bf16 v[30:33], a[28:31], v[22:25], v[30:33]\n"
      "v_mfma_f32_16x16x32_bf16 v[34:37], a[32:35], v[10:13], 0\n"
      "s_mov_b32 s66, 0x00030003\n"
      "s_mov_b32 s67, 0x00030003\n"
      "s_nop 0\n"
      "v_cndmask_b32 v38, v38, v26, s[66:67]\n"
      "v_mfma_f32_16x16x32_bf16 v[34:37], a[36:39], v[14:17], v[34:37]\n"
      "v_cndmask_b32 v39, v39, v27, s[66:67]\n"
      "v_mfma_f32_16x16x32_bf16 v[34:37], a[40:43], v[18:21], v[34:37]\n"
      "v_cndmask_b32 v40, v40, v28, s[66:67]\n"
      "v_mfma_f32_16x16x32_bf16 v[34:37], a[44:47], v[22:25], v[34:37]\n"
      "v_cndmask_b32 v41, v41, v29, s[66:67]\n"
      "v_mfma_f32_16x16x32_bf16 v[26:29], a[48:51], v[10:13], 0\n"
      "v_cndmask_b32 v42, v42, v30, s[66:67]\n"
      "v_mfma_f32_16x16x32_bf16 v[26:29], a[52:55], v[14:17], v[26:29]\n"
      "v_cndmask_b32 v43, v43, v31, s[66:67]\n"
      "v_mfma_f32_16x16x32_bf16 v[26:29], a[56:59], v[18:21], v[26:29]\n"
      "v_cndmask_b32 v44, v44, v32, s[66:67]\n"
      "v_mfma_f32_16x16x32_bf16 v[26:29], a[60:63], v[22:25], v[26:29]\n"
      "v_cndmask_b32 v45, v45, v33, s[66:67]\n"
      "v_mfma_f32_16x16x32_bf16 v[30:33], a[64:67], v[10:13], 0\n"
      "s_lshl_b64 s[66:67], s[66:67], 2\n"
      "v_cndmask_b32 v38, v38, v34, s[66:67]\n"
      "v_mfma_f32_16x16x32_bf16 v[30:33], a[68:71], v[14:17], v[30:33]\n"
      "v_cndmask_b32 v39, v39, v35, s[66:67]\n"
      "v_mfma_f32_16x16x32_bf16 v[30:33], a[72:75], v[18:21], v[30:33]\n"
      "v_cndmask_b32 v40, v40, v36, s[66:67]\n"
      "v_mfma_f32_16x16x32_bf16 v[30:33], a[76:79], v[22:25], v[30:33]\n"
      "v_cndmask_b32 v41, v41, v37, s[66:67]\n"
      "v_mfma_f32_16x16x32_bf16 v[34:37], a[80:83], v[10:13], 0\n"
      "v_cndmask_b32 v42, v42, v26, s[66:67]\n"
      "v_mfma_f32_16x16x32_bf16 v[34:37], a[84:87], v[14:17], v[34:37]\n"
      "v_cndmask_b32 v43, v43, v27, s[66:67]\n"
      "v_mfma_f32_16x16x32_bf16 v[34:37], a[88:91], v[18:21], v[34:37]\n"
      "v_cndmask_b32 v44, v44, v28, s[66:67]\n"
      "v_mfma_f32_16x16x32_bf16 v[34:37], a[92:95], v[22:25], v[34:37]\n"
      "v_cndmask_b32 v45, v45, v29, s[66:67]\n"
      "v_mfma_f32_16x16x32_bf16 v[26:29], a[96:99], v[10:13], 0\n"
      "s_lshl_b64 s[66:67], s[66:67], 2\n"
      "v_cndmask_b32 v38, v38, v30, s[66:67]\n"
      "v_mfma_f32_16x16x32_bf16 v[26:29], a[100:103], v[14:17], v[26:29]\n"
      "v_cndmask_b32 v39, v39, v31, s[66:67]\n"
      "v_mfma_f32_16x16x32_bf16 v[26:29], a[104:107], v[18:21], v[26:29]\n"
      "v_cndmask_b32 v40, v40, v32, s[66:67]\n"
      "v_mfma_f32_16x16x32_bf16 v[26:29], a[108:111], v[22:25], v[26:29]\n"
      "v_cndmask_b32 v41, v41, v33, s[66:67]\n"
      "v_mfma_f32_16x16x32_bf16 v[30:33], a[112:115], v[10:13], 0\n"
      "v_cndmask_b32 v42, v42, v34, s[66:67]\n"
      "v_mfma_f32_16x16x32_bf16 v[30:33], a[116:119], v[14:17], v[30:33]\n"
      "v_cndmask_b32 v43, v43, v35, s[66:67]\n"
      "v_mfma_f32_16x16x32_bf16 v[30:33], a[120:123], v[18:21], v[30:33]\n"
      "v_cndmask_b32 v44, v44, v36, s[66:67]\n"
      "v_mfma_f32_16x16x32_bf16 v[30:33], a[124:127], v[22:25], v[30:33]\n"
      "v_cndmask_b32 v45, v45, v37, s[66:67]\n"
      "v_mfma_f32_16x16x32_bf16 v[34:37], a[128:131], v[10:13], 0\n"
      "s_lshl_b64 s[66:67], s[66:67], 2\n"
      "v_cndmask_b32 v38, v38, v26, s[66:67]\n"
      "v_mfma_f32_16x16x32_bf16 v[34:37], a[132:135], v[14:17], v[34:37]\n"
      "v_cndmask_b32 v39, v39, v27, s[66:67]\n"
      "v_mfma_f32_16x16x32_bf16 v[34:37], a[136:139], v[18:21], v[34:37]\n"
      "v_cndmask_b32 v40, v40, v28, s[66:67]\n"
      "v_mfma_f32_16x16x32_bf16 v[34:37], a[140:143], v[22:25], v[34:37]\n"
      "v_cndmask_b32 v41, v41, v29, s[66:67]\n"
      "v_mfma_f32_16x16x32_bf16 v[26:29], a[144:147], v[10:13], 0\n"
      "v_cndmask_b32 v42, v42, v30, s[66:67]\n"
      "v_mfma_f32_16x16x32_bf16 v[26:29], a[148:151], v[14:17], v[26:29]\n"
      "v_cndmask_b32 v43, v43, v31, s[66:67]\n"
      "v_mfma_f32_16x16x32_bf16 v[26:29], a[152:155], v[18:21], v[26:29]\n"
      "v_cndmask_b32 v44, v44, v32, s[66:67]\n"
      "v_mfma_f32_16x16x32_bf16 v[26:29], a[156:159], v[22:25], v[26:29]\n"
      "v_cndmask_b32 v45, v45, v33, s[66:67]\n"
      "v_mfma_f32_16x16x32_bf16 v[30:33], a[160:163], v[10:13], 0\n"
      "s_lshl_b64 s[66:67], s[66:67], 2\n"
      "v_cndmask_b32 v38, v38, v34, s[66:67]\n"
      "v_mfma_f32_16x16x32_bf16 v[30:33], a[164:167], v[14:17], v[30:33]\n"
      "v_cndmask_b32 v39, v39, v35, s[66:67]\n"
      "v_mfma_f32_16x16x32_bf16 v[30:33], a[168:171], v[18:21], v[30:33]\n"
      "v_cndmask_b32 v40, v40, v36, s[66:67]\n"
      "v_mfma_f32_16x16x32_bf16 v[30:33], a[172:175], v[22:25], v[30:33]\n"
      "v_cndmask_b32 v41, v41, v37, s[66:67]\n"
      "v_mfma_f32_16x16x32_bf16 v[34:37], a[176:179], v[10:13], 0\n"
      "v_cndmask_b32 v42, v42, v26, s[66:67]\n"
      "v_mfma_f32_16x16x32_bf16 v[34:37], a[180:183], v[14:17], v[34:37]\n"
      "v_cndmask_b32 v43, v43, v27, s[66:67]\n"
      "v_mfma_f32_16x16x32_bf16 v[34:37], a[184:187], v[18:21], v[34:37]\n"
      "v_cndmask_b32 v44, v44, v28, s[66:67]\n"
      "v_mfma_f32_16x16x32_bf16 v[34:37], a[188:191], v[22:25], v[34:37]\n"
      "v_cndmask_b32 v45, v45, v29, s[66:67]\n"
      "v_mfma_f32_16x16x32_bf16 v[26:29], a[192:195], v[10:13], 0\n"
      "s_lshl_b64 s[66:67], s[66:67], 2\n"
      "v_cndmask_b32 v38, v38, v30, s[66:67]\n"
      "v_mfma_f32_16x16x32_bf16 v[26:29], a[196:199], v[14:17], v[26:29]\n"
      "v_cndmask_b32 v39, v39, v31, s[66:67]\n"
      "v_mfma_f32_16x16x32_bf16 v[26:29], a[200:203], v[18:21], v[26:29]\n"
      "v_cndmask_b32 v40, v40, v32, s[66:67]\n"
      "v_mfma_f32_16x16x32_bf16 v[26:29], a[204:207], v[22:25], v[26:29]\n"
      "v_cndmask_b32 v41, v41, v33, s[66:67]\n"
      "v_mfma_f32_16x16x32_bf16 v[30:33], a[208:211], v[10:13], 0\n"
      "v_cndmask_b32 v42, v42, v34, s[66:67]\n"
      "v_mfma_f32_16x16x32_bf16 v[30:33], a[212:215], v[14:17], v[30:33]\n"
      "v_cndmask_b32 v43, v43, v35, s[66:67]\n"
      "v_mfma_f32_16x16x32_bf16 v[30:33], a[216:219], v[18:21], v[30:33]\n"
      "v_cndmask_b32 v44, v44, v36, s[66:67]\n"
      "v_mfma_f32_16x16x32_bf16 v[30:33], a[220:223], v[22:25], v[30:33]\n"
      "v_cndmask_b32 v45, v45, v37, s[66:67]\n"
      "v_mfma_f32_16x16x32_bf16 v[34:37], a[224:227], v[10:13], 0\n"
      "s_lshl_b64 s[66:67], s[66:67], 2\n"
      "v_cndmask_b32 v38, v38, v26, s[66:67]\n"
      "v_mfma_f32_16x16x32_bf16 v[34:37], a[228:231], v[14:17], v[34:37]\n"
      "v_cndmask_b32 v39, v39, v27, s[66:67]\n"
      "v_mfma_f32_16x16x32_bf16 v[34:37], a[232:235], v[18:21], v[34:37]\n"
      "v_cndmask_b32 v40, v40, v28, s[66:67]\n"
      "v_mfma_f32_16x16x32_bf16 v[34:37], a[236:239], v[22:25], v[34:37]\n"
      "v_cndmask_b32 v41, v41, v29, s[66:67]\n"
      "v_mfma_f32_16x16x32_bf16 v[26:29], a[240:243], v[10:13], 0\n"
      "v_cndmask_b32 v42, v42, v30, s[66:67]\n"
      "v_mfma_f32_16x16x32_bf16 v[26:29], a[244:247], v[14:17], v[26:29]\n"
      "v_cndmask_b32 v43, v43, v31, s[66:67]\n"
      "v_mfma_f32_16x16x32_bf16 v[26:29], a[248:251], v[18:21], v[26:29]\n"
      "v_cndmask_b32 v44, v44, v32, s[66:67]\n"
      "v_mfma_f32_16x16x32_bf16 v[26:29], a[252:255], v[22:25], v[26:29]\n"
      "v_cndmask_b32 v45, v45, v33, s[66:67]\n"
      "s_lshl_b64 s[66:67], s[66:67], 2\n"
      "v_cndmask_b32 v38, v38, v34, s[66:67]\n"
      "v_cndmask_b32 v39, v39, v35, s[66:67]\n"
      "v_cndmask_b32 v40, v40, v36, s[66:67]\n"
      "v_cndmask_b32 v41, v41, v37, s[66:67]\n"
      "v_mov_b32 v10, 0\n"
      "v_mov_b32 v11, 0\n"
      "v_cndmask_b32 v42, v42, v26, s[66:67]\n"
      "v_cndmask_b32 v43, v43, v27, s[66:67]\n"
      "v_cndmask_b32 v44, v44, v28, s[66:67]\n"
      "v_cndmask_b32 v45, v45, v29, s[66:67]\n"
      "v_max3_f32 v46, v38, v39, v40\n"
      "v_max3_f32 v26, v41, v42, v43\n"
      "v_max3_f32 v46, v46, v44, v45\n"
      "v_max_f32 v46, v46, v26\n"
      "v_mov_b32 v12, 0\n"
      "v_mov_b32 v13, 0\n"
      "v_max_f32_dpp v46, v46, v46 row_ror:2 row_mask:0xf bank_mask:0xf\n"
      "v_mov_b32 v14, 0\n"
      "v_mov_b32 v15, 0\n"
      "v_max_f32_dpp v46, v46, v46 row_ror:4 row_mask:0xf bank_mask:0xf\n"
      "v_mov_b32 v16, 0\n"
      "v_mov_b32 v17, 0\n"
      "v_max_f32_dpp v46, v46, v46 row_ror:8 row_mask:0xf bank_mask:0xf\n"
      "v_mov_b32 v26, v46\n"
      "v_mov_b32 v18, 0\n"
      "v_mov_b32 v19, 0\n"
      "v_permlane16_swap_b32 v46, v26\n"
      "v_max_f32 v46, v46, v26\n"
      "v_mov_b32 v26, v46\n"
      "v_mov_b32 v20, 0\n"
      "v_mov_b32 v21, 0\n"
      "v_permlane32_swap_b32 v46, v26\n"
      "v_max_f32 v46, v46, v26\n"
      "v_mul_f32 v46, s71, v46\n"
      "v_fma_f32 v38, v38, s71, -v46\n"
      "v_fma_f32 v39, v39, s71, -v46\n"
      "v_fma_f32 v40, v40, s71, -v46\n"
      "v_fma_f32 v41, v41, s71, -v46\n"
      "v_fma_f32 v42, v42, s71, -v46\n"
      "v_fma_f32 v43, v43, s71, -v46\n"
      "v_fma_f32 v44, v44, s71, -v46\n"
      "v_fma_f32 v45, v45, s71, -v46\n"
      "v_exp_f32 v38, v38\n"
      "v_exp_f32 v39, v39\n"
      "v_exp_f32 v40, v40\n"
      "v_exp_f32 v41, v41\n"
      "v_exp_f32 v42, v42\n"
      "v_exp_f32 v43, v43\n"
      "v_exp_f32 v44, v44\n"
      "v_exp_f32 v45, v45\n"
      "v_pk_add_f32 v[26:27], v[38:39], v[40:41]\n"
      "v_pk_add_f32 v[28:29], v[42:43], v[44:45]\n"
      "v_cvt_pk_bf16_f32 v38, v38, v39\n"
      "v_cvt_pk_bf16_f32 v39, v40, v41\n"
      "v_cvt_pk_bf16_f32 v40, v42, v43\n"
      "v_cvt_pk_bf16_f32 v41, v44, v45\n"
      "v_pk_add_f32 v[26:27], v[26:27], v[28:29]\n"
      "v_add_f32 v47, v26, v27\n"
      "v_mov_b32 v22, 0\n"
      "v_mov_b32 v23, 0\n"
      "v_mov_b32 v24, 0\n"
      "v_mov_b32 v25, 0\n"
      "ds_read_b64_tr_b16 v[26:27], v6 offset:8192\n"
      "ds_read_b64_tr_b16 v[28:29], v6 offset:12288\n"
      "v_xor_b32 v8, 0x20, v6\n"
      "ds_read_b64_tr_b16 v[30:31], v8 offset:8192\n"
      "ds_read_b64_tr_b16 v[32:33], v8 offset:12288\n"
      "v_xor_b32 v7, 0x40, v6\n"
      "ds_read_b64_tr_b16 v[34:35], v7 offset:8192\n"
      "ds_read_b64_tr_b16 v[36:37], v7 offset:12288\n"
      "v_xor_b32 v8, 0x60, v6\n"
      "ds_read_b64_tr_b16 v[42:43], v8 offset:8192\n"
      "ds_read_b64_tr_b16 v[44:45], v8 offset:12288\n"
      "v_mov_b32_dpp v10, v38 quad_perm:[0,1,2,3] row_mask:0xf bank_mask:0x1\n"
      "v_mov_b32_dpp v11, v39 quad_perm:[0,1,2,3] row_mask:0xf bank_mask:0x1\n"
      "v_mov_b32_dpp v12, v40 quad_perm:[0,1,2,3] row_mask:0xf bank_mask:0x1\n"
      "v_mov_b32_dpp v13, v41 quad_perm:[0,1,2,3] row_mask:0xf bank_mask:0x1\n"
      "s_nop 1\n"
      "v_mfma_f32_16x16x32_bf16 v[48:51], v[10:13], v[96:99], 0\n"
      "v_mov_b32_dpp v14, v38 row_shr:4 row_mask:0xf bank_mask:0x2\n"
      "v_mov_b32_dpp v15, v39 row_shr:4 row_mask:0xf bank_mask:0x2\n"
      "v_mov_b32_dpp v16, v40 row_shr:4 row_mask:0xf bank_mask:0x2\n"
      "v_mov_b32_dpp v17, v41 row_shr:4 row_mask:0xf bank_mask:0x2\n"
      "v_mfma_f32_16x16x32_bf16 v[52:55], v[10:13], v[100:103], 0\n"
      "s_nop 0\n"
      "v_mfma_f32_16x16x32_bf16 v[48:51], v[14:17], v[104:107], v[48:51]\n"
      "v_mov_b32_dpp v10, v38 row_shl:10 row_mask:0xf bank_mask:0x1\n"
      "v_mov_b32_dpp v11, v39 row_shl:10 row_mask:0xf bank_mask:0x1\n"
      "v_mov_b32_dpp v12, v40 row_shl:10 row_mask:0xf bank_mask:0x1\n"
      "v_mov_b32_dpp v13, v41 row_shl:10 row_mask:0xf bank_mask:0x1\n"
      "v_mfma_f32_16x16x32_bf16 v[52:55], v[14:17], v[108:111], v[52:55]\n"
      "v_add_f32_dpp v47, v47, v47 row_ror:2 row_mask:0xf bank_mask:0xf\n"
      "s_nop 1\n"
      "v_add_f32_dpp v47, v47, v47 row_ror:4 row_mask:0xf bank_mask:0xf\n"
      "s_nop 1\n"
      "v_add_f32_dpp v47, v47, v47 row_ror:8 row_mask:0xf bank_mask:0xf\n"
      "v_mov_b32 v1, v47\n"
      "s_nop 1\n"
      "v_permlane16_swap_b32 v47, v1\n"
      "v_add_f32 v47, v47, v1\n"
      "v_mov_b32 v1, v47\n"
      "s_nop 1\n"
      "v_permlane32_swap_b32 v47, v1\n"
      "v_add_f32 v47, v47, v1\n"
      "s_waitcnt lgkmcnt(6)\n"
      "v_mfma_f32_16x16x32_bf16 v[48:51], v[10:13], v[26:29], v[48:51]\n"
      "v_mov_b32_dpp v18, v38 row_shr:8 row_mask:0xf bank_mask:0x4\n"
      "v_mov_b32_dpp v19, v39 row_shr:8 row_mask:0xf bank_mask:0x4\n"
      "v_mov_b32_dpp v20, v40 row_shr:8 row_mask:0xf bank_mask:0x4\n"
      "v_mov_b32_dpp v21, v41 row_shr:8 row_mask:0xf bank_mask:0x4\n"
      "s_waitcnt lgkmcnt(4)\n"
      "v_mfma_f32_16x16x32_bf16 v[52:55], v[10:13], v[30:33], v[52:55]\n"
      "v_xor_b32 v7, 0x80, v6\n"
      "ds_read_b64_tr_b16 v[26:27], v7 offset:8192\n"
      "ds_read_b64_tr_b16 v[28:29], v7 offset:12288\n"
      "v_xor_b32 v8, 0xa0, v6\n"
      "ds_read_b64_tr_b16 v[30:31], v8 offset:8192\n"
      "ds_read_b64_tr_b16 v[32:33], v8 offset:12288\n"
      "v_mfma_f32_16x16x32_bf16 v[48:51], v[18:21], v[112:115], v[48:51]\n"
      "v_mov_b32_dpp v22, v38 row_shr:12 row_mask:0xf bank_mask:0x8\n"
      "v_mov_b32_dpp v23, v39 row_shr:12 row_mask:0xf bank_mask:0x8\n"
      "v_mov_b32_dpp v24, v40 row_shr:12 row_mask:0xf bank_mask:0x8\n"
      "v_mov_b32_dpp v25, v41 row_shr:12 row_mask:0xf bank_mask:0x8\n"
      "v_mfma_f32_16x16x32_bf16 v[52:55], v[18:21], v[116:119], v[52:55]\n"
      "s_nop 0\n"
      "v_mfma_f32_16x16x32_bf16 v[48:51], v[22:25], v[120:123], v[48:51]\n"
      "v_mov_b32_dpp v14, v38 row_shl:6 row_mask:0xf bank_mask:0x2\n"
      "v_mov_b32_dpp v15, v39 row_shl:6 row_mask:0xf bank_mask:0x2\n"
      "v_mov_b32_dpp v16, v40 row_shl:6 row_mask:0xf bank_mask:0x2\n"
      "v_mov_b32_dpp v17, v41 row_shl:6 row_mask:0xf bank_mask:0x2\n"
      "v_mfma_f32_16x16x32_bf16 v[52:55], v[22:25], v[124:127], v[52:55]\n"
      "s_waitcnt lgkmcnt(6)\n"
      "v_mfma_f32_16x16x32_bf16 v[48:51], v[14:17], v[34:37], v[48:51]\n"
      "v_mov_b32_dpp v10, v38 row_shl:2 row_mask:0xf bank_mask:0x1\n"
      "v_mov_b32_dpp v11, v39 row_shl:2 row_mask:0xf bank_mask:0x1\n"
      "v_mov_b32_dpp v12, v40 row_shl:2 row_mask:0xf bank_mask:0x1\n"
      "v_mov_b32_dpp v13, v41 row_shl:2 row_mask:0xf bank_mask:0x1\n"
      "s_waitcnt lgkmcnt(4)\n"
      "v_mfma_f32_16x16x32_bf16 v[52:55], v[14:17], v[42:45], v[52:55]\n"
      "v_xor_b32 v7, 0xc0, v6\n"
      "ds_read_b64_tr_b16 v[34:35], v7 offset:8192\n"
      "ds_read_b64_tr_b16 v[36:37], v7 offset:12288\n"
      "v_xor_b32 v8, 0xe0, v6\n"
      "ds_read_b64_tr_b16 v[42:43], v8 offset:8192\n"
      "ds_read_b64_tr_b16 v[44:45], v8 offset:12288\n"
      "v_mfma_f32_16x16x32_bf16 v[48:51], v[10:13], v[128:131], v[48:51]\n"
      "v_mov_b32_dpp v14, v38 row_shr:2 row_mask:0xf bank_mask:0x2\n"
      "v_mov_b32_dpp v15, v39 row_shr:2 row_mask:0xf bank_mask:0x2\n"
      "v_mov_b32_dpp v16, v40 row_shr:2 row_mask:0xf bank_mask:0x2\n"
      "v_mov_b32_dpp v17, v41 row_shr:2 row_mask:0xf bank_mask:0x2\n"
      "v_mfma_f32_16x16x32_bf16 v[52:55], v[10:13], v[132:135], v[52:55]\n"
      "s_nop 0\n"
      "v_mfma_f32_16x16x32_bf16 v[48:51], v[14:17], v[136:139], v[48:51]\n"
      "v_mov_b32_dpp v18, v38 row_shl:2 row_mask:0xf bank_mask:0x4\n"
      "v_mov_b32_dpp v19, v39 row_shl:2 row_mask:0xf bank_mask:0x4\n"
      "v_mov_b32_dpp v20, v40 row_shl:2 row_mask:0xf bank_mask:0x4\n"
      "v_mov_b32_dpp v21, v41 row_shl:2 row_mask:0xf bank_mask:0x4\n"
      "v_mfma_f32_16x16x32_bf16 v[52:55], v[14:17], v[140:143], v[52:55]\n"
      "s_waitcnt lgkmcnt(6)\n"
      "v_mfma_f32_16x16x32_bf16 v[48:51], v[18:21], v[26:29], v[48:51]\n"
      "s_waitcnt lgkmcnt(4)\n"
      "v_mfma_f32_16x16x32_bf16 v[52:55], v[18:21], v[30:33], v[52:55]\n"
      "v_mov_b32_dpp v18, v38 row_shr:6 row_mask:0xf bank_mask:0x4\n"
      "v_mov_b32_dpp v19, v39 row_shr:6 row_mask:0xf bank_mask:0x4\n"
      "v_mov_b32_dpp v20, v40 row_shr:6 row_mask:0xf bank_mask:0x4\n"
      "v_mov_b32_dpp v21, v41 row_shr:6 row_mask:0xf bank_mask:0x4\n"
      "ds_read_b64_tr_b16 v[26:27], v6 offset:16384\n"
      "ds_read_b64_tr_b16 v[28:29], v6 offset:20480\n"
      "v_xor_b32 v8, 0x20, v6\n"
      "ds_read_b64_tr_b16 v[30:31], v8 offset:16384\n"
      "ds_read_b64_tr_b16 v[32:33], v8 offset:20480\n"
      "v_mfma_f32_16x16x32_bf16 v[48:51], v[18:21], v[144:147], v[48:51]\n"
      "v_mov_b32_dpp v22, v38 row_shr:10 row_mask:0xf bank_mask:0x8\n"
      "v_mov_b32_dpp v23, v39 row_shr:10 row_mask:0xf bank_mask:0x8\n"
      "v_mov_b32_dpp v24, v40 row_shr:10 row_mask:0xf bank_mask:0x8\n"
      "v_mov_b32_dpp v25, v41 row_shr:10 row_mask:0xf bank_mask:0x8\n"
      "v_mfma_f32_16x16x32_bf16 v[52:55], v[18:21], v[148:151], v[52:55]\n"
      "s_nop 0\n"
      "v_mfma_f32_16x16x32_bf16 v[48:51], v[22:25], v[152:155], v[48:51]\n"
      "v_mfma_f32_16x16x32_bf16 v[52:55], v[22:25], v[156:159], v[52:55]\n"
      "v_mov_b32_dpp v22, v38 row_shr:2 row_mask:0xf bank_mask:0x8\n"
      "v_mov_b32_dpp v23, v39 row_shr:2 row_mask:0xf bank_mask:0x8\n"
      "v_mov_b32_dpp v24, v40 row_shr:2 row_mask:0xf bank_mask:0x8\n"
      "v_mov_b32_dpp v25, v41 row_shr:2 row_mask:0xf bank_mask:0x8\n"
      "s_waitcnt lgkmcnt(6)\n"
      "s_nop 0\n"
      "v_mfma_f32_16x16x32_bf16 v[48:51], v[22:25], v[34:37], v[48:51]\n"
      "v_mov_b32_dpp v10, v38 row_shl:4 row_mask:0xf bank_mask:0x1\n"
      "v_mov_b32_dpp v11, v39 row_shl:4 row_mask:0xf bank_mask:0x1\n"
      "v_mov_b32_dpp v12, v40 row_shl:4 row_mask:0xf bank_mask:0x1\n"
      "v_mov_b32_dpp v13, v41 row_shl:4 row_mask:0xf bank_mask:0x1\n"
      "s_waitcnt lgkmcnt(4)\n"
      "v_mfma_f32_16x16x32_bf16 v[52:55], v[22:25], v[42:45], v[52:55]\n"
      "v_xor_b32 v7, 0x40, v6\n"
      "ds_read_b64_tr_b16 v[34:35], v7 offset:16384\n"
      "ds_read_b64_tr_b16 v[36:37], v7 offset:20480\n"
      "v_xor_b32 v8, 0x60, v6\n"
      "ds_read_b64_tr_b16 v[42:43], v8 offset:16384\n"
      "ds_read_b64_tr_b16 v[44:45], v8 offset:20480\n"
      "v_mfma_f32_16x16x32_bf16 v[48:51], v[10:13], v[160:163], v[48:51]\n"
      "v_mov_b32_dpp v14, v38 quad_perm:[0,1,2,3] row_mask:0xf bank_mask:0x2\n"
      "v_mov_b32_dpp v15, v39 quad_perm:[0,1,2,3] row_mask:0xf bank_mask:0x2\n"
      "v_mov_b32_dpp v16, v40 quad_perm:[0,1,2,3] row_mask:0xf bank_mask:0x2\n"
      "v_mov_b32_dpp v17, v41 quad_perm:[0,1,2,3] row_mask:0xf bank_mask:0x2\n"
      "v_mfma_f32_16x16x32_bf16 v[52:55], v[10:13], v[164:167], v[52:55]\n"
      "s_nop 0\n"
      "v_mfma_f32_16x16x32_bf16 v[48:51], v[14:17], v[168:171], v[48:51]\n"
      "v_mov_b32_dpp v10, v38 row_shl:12 row_mask:0xf bank_mask:0x1\n"
      "v_mov_b32_dpp v11, v39 row_shl:12 row_mask:0xf bank_mask:0x1\n"
      "v_mov_b32_dpp v12, v40 row_shl:12 row_mask:0xf bank_mask:0x1\n"
      "v_mov_b32_dpp v13, v41 row_shl:12 row_mask:0xf bank_mask:0x1\n"
      "v_mfma_f32_16x16x32_bf16 v[52:55], v[14:17], v[172:175], v[52:55]\n"
      "s_waitcnt lgkmcnt(6)\n"
      "v_mfma_f32_16x16x32_bf16 v[48:51], v[10:13], v[26:29], v[48:51]\n"
      "v_mov_b32_dpp v18, v38 row_shr:4 row_mask:0xf bank_mask:0x4\n"
      "v_mov_b32_dpp v19, v39 row_shr:4 row_mask:0xf bank_mask:0x4\n"
      "v_mov_b32_dpp v20, v40 row_shr:4 row_mask:0xf bank_mask:0x4\n"
      "v_mov_b32_dpp v21, v41 row_shr:4 row_mask:0xf bank_mask:0x4\n"
      "s_waitcnt lgkmcnt(4)\n"
      "v_mfma_f32_16x16x32_bf16 v[52:55], v[10:13], v[30:33], v[52:55]\n"
      "v_xor_b32 v7, 0x80, v6\n"
      "ds_read_b64_tr_b16 v[26:27], v7 offset:16384\n"
      "ds_read_b64_tr_b16 v[28:29], v7 offset:20480\n"
      "v_xor_b32 v8, 0xa0, v6\n"
      "ds_read_b64_tr_b16 v[30:31], v8 offset:16384\n"
      "ds_read_b64_tr_b16 v[32:33], v8 offset:20480\n"
      "v_mfma_f32_16x16x32_bf16 v[48:51], v[18:21], v[176:179], v[48:51]\n"
      "v_mov_b32_dpp v22, v38 row_shr:8 row_mask:0xf bank_mask:0x8\n"
      "v_mov_b32_dpp v23, v39 row_shr:8 row_mask:0xf bank_mask:0x8\n"
      "v_mov_b32_dpp v24, v40 row_shr:8 row_mask:0xf bank_mask:0x8\n"
      "v_mov_b32_dpp v25, v41 row_shr:8 row_mask:0xf bank_mask:0x8\n"
      "v_mfma_f32_16x16x32_bf16 v[52:55], v[18:21], v[180:183], v[52:55]\n"
      "s_nop 0\n"
      "v_mfma_f32_16x16x32_bf16 v[48:51], v[22:25], v[184:187], v[48:51]\n"
      "v_mov_b32_dpp v14, v38 row_shl:8 row_mask:0xf bank_mask:0x2\n"
      "v_mov_b32_dpp v15, v39 row_shl:8 row_mask:0xf bank_mask:0x2\n"
      "v_mov_b32_dpp v16, v40 row_shl:8 row_mask:0xf bank_mask:0x2\n"
      "v_mov_b32_dpp v17, v41 row_shl:8 row_mask:0xf bank_mask:0x2\n"
      "v_mfma_f32_16x16x32_bf16 v[52:55], v[22:25], v[188:191], v[52:55]\n"
      "s_waitcnt lgkmcnt(6)\n"
      "v_mfma_f32_16x16x32_bf16 v[48:51], v[14:17], v[34:37], v[48:51]\n"
      "v_mov_b32_dpp v10, v38 row_shl:6 row_mask:0xf bank_mask:0x1\n"
      "v_mov_b32_dpp v11, v39 row_shl:6 row_mask:0xf bank_mask:0x1\n"
      "v_mov_b32_dpp v12, v40 row_shl:6 row_mask:0xf bank_mask:0x1\n"
      "v_mov_b32_dpp v13, v41 row_shl:6 row_mask:0xf bank_mask:0x1\n"
      "s_waitcnt lgkmcnt(4)\n"
      "v_mfma_f32_16x16x32_bf16 v[52:55], v[14:17], v[42:45], v[52:55]\n"
      "v_xor_b32 v7, 0xc0, v6\n"
      "ds_read_b64_tr_b16 v[34:35], v7 offset:16384\n"
      "ds_read_b64_tr_b16 v[36:37], v7 offset:20480\n"
      "v_xor_b32 v8, 0xe0, v6\n"
      "ds_read_b64_tr_b16 v[42:43], v8 offset:16384\n"
      "ds_read_b64_tr_b16 v[44:45], v8 offset:20480\n"
      "v_mfma_f32_16x16x32_bf16 v[48:51], v[10:13], v[192:195], v[48:51]\n"
      "v_mov_b32_dpp v14, v38 row_shl:2 row_mask:0xf bank_mask:0x2\n"
      "v_mov_b32_dpp v15, v39 row_shl:2 row_mask:0xf bank_mask:0x2\n"
      "v_mov_b32_dpp v16, v40 row_shl:2 row_mask:0xf bank_mask:0x2\n"
      "v_mov_b32_dpp v17, v41 row_shl:2 row_mask:0xf bank_mask:0x2\n"
      "v_mfma_f32_16x16x32_bf16 v[52:55], v[10:13], v[196:199], v[52:55]\n"
      "s_nop 0\n"
      "v_mfma_f32_16x16x32_bf16 v[48:51], v[14:17], v[200:203], v[48:51]\n"
      "v_mov_b32_dpp v18, v38 row_shl:4 row_mask:0xf bank_mask:0x4\n"
      "v_mov_b32_dpp v19, v39 row_shl:4 row_mask:0xf bank_mask:0x4\n"
      "v_mov_b32_dpp v20, v40 row_shl:4 row_mask:0xf bank_mask:0x4\n"
      "v_mov_b32_dpp v21, v41 row_shl:4 row_mask:0xf bank_mask:0x4\n"
      "v_mfma_f32_16x16x32_bf16 v[52:55], v[14:17], v[204:207], v[52:55]\n"
      "s_waitcnt lgkmcnt(6)\n"
      "v_mfma_f32_16x16x32_bf16 v[48:51], v[18:21], v[26:29], v[48:51]\n"
      "s_waitcnt lgkmcnt(4)\n"
      "v_mfma_f32_16x16x32_bf16 v[52:55], v[18:21], v[30:33], v[52:55]\n"
      "v_mov_b32_dpp v18, v38 row_shr:2 row_mask:0xf bank_mask:0x4\n"
      "v_mov_b32_dpp v19, v39 row_shr:2 row_mask:0xf bank_mask:0x4\n"
      "v_mov_b32_dpp v20, v40 row_shr:2 row_mask:0xf bank_mask:0x4\n"
      "v_mov_b32_dpp v21, v41 row_shr:2 row_mask:0xf bank_mask:0x4\n"
      "ds_read_b64_tr_b16 v[26:27], v6 offset:0\n"
      "ds_read_b64_tr_b16 v[28:29], v6 offset:4096\n"
      "v_xor_b32 v8, 0x20, v6\n"
      "ds_read_b64_tr_b16 v[30:31], v8 offset:0\n"
      "ds_read_b64_tr_b16 v[32:33], v8 offset:4096\n"
      "v_mfma_f32_16x16x32_bf16 v[48:51], v[18:21], v[208:211], v[48:51]\n"
      "v_mov_b32_dpp v22, v38 row_shr:6 row_mask:0xf bank_mask:0x8\n"
      "v_mov_b32_dpp v23, v39 row_shr:6 row_mask:0xf bank_mask:0x8\n"
      "v_mov_b32_dpp v24, v40 row_shr:6 row_mask:0xf bank_mask:0x8\n"
      "v_mov_b32_dpp v25, v41 row_shr:6 row_mask:0xf bank_mask:0x8\n"
      "v_mfma_f32_16x16x32_bf16 v[52:55], v[18:21], v[212:215], v[52:55]\n"
      "s_nop 0\n"
      "v_mfma_f32_16x16x32_bf16 v[48:51], v[22:25], v[216:219], v[48:51]\n"
      "v_mfma_f32_16x16x32_bf16 v[52:55], v[22:25], v[220:223], v[52:55]\n"
      "v_mov_b32_dpp v22, v38 quad_perm:[0,1,2,3] row_mask:0xf bank_mask:0x8\n"
      "v_mov_b32_dpp v23, v39 quad_perm:[0,1,2,3] row_mask:0xf bank_mask:0x8\n"
      "v_mov_b32_dpp v24, v40 quad_perm:[0,1,2,3] row_mask:0xf bank_mask:0x8\n"
      "v_mov_b32_dpp v25, v41 quad_perm:[0,1,2,3] row_mask:0xf bank_mask:0x8\n"
      "s_waitcnt lgkmcnt(6)\n"
      "s_nop 0\n"
      "v_mfma_f32_16x16x32_bf16 v[48:51], v[22:25], v[34:37], v[48:51]\n"
      "v_mov_b32_dpp v10, v38 row_shl:8 row_mask:0xf bank_mask:0x1\n"
      "v_mov_b32_dpp v11, v39 row_shl:8 row_mask:0xf bank_mask:0x1\n"
      "v_mov_b32_dpp v12, v40 row_shl:8 row_mask:0xf bank_mask:0x1\n"
      "v_mov_b32_dpp v13, v41 row_shl:8 row_mask:0xf bank_mask:0x1\n"
      "s_waitcnt lgkmcnt(4)\n"
      "v_mfma_f32_16x16x32_bf16 v[52:55], v[22:25], v[42:45], v[52:55]\n"
      "v_xor_b32 v7, 0x40, v6\n"
      "ds_read_b64_tr_b16 v[34:35], v7 offset:0\n"
      "ds_read_b64_tr_b16 v[36:37], v7 offset:4096\n"
      "v_xor_b32 v8, 0x60, v6\n"
      "ds_read_b64_tr_b16 v[42:43], v8 offset:0\n"
      "ds_read_b64_tr_b16 v[44:45], v8 offset:4096\n"
      "v_mfma_f32_16x16x32_bf16 v[48:51], v[10:13], v[224:227], v[48:51]\n"
      "v_mov_b32_dpp v14, v38 row_shl:4 row_mask:0xf bank_mask:0x2\n"
      "v_mov_b32_dpp v15, v39 row_shl:4 row_mask:0xf bank_mask:0x2\n"
      "v_mov_b32_dpp v16, v40 row_shl:4 row_mask:0xf bank_mask:0x2\n"
      "v_mov_b32_dpp v17, v41 row_shl:4 row_mask:0xf bank_mask:0x2\n"
      "v_mfma_f32_16x16x32_bf16 v[52:55], v[10:13], v[228:231], v[52:55]\n"
      "s_nop 0\n"
      "v_mfma_f32_16x16x32_bf16 v[48:51], v[14:17], v[232:235], v[48:51]\n"
      "v_mov_b32_dpp v10, v38 row_shl:14 row_mask:0xf bank_mask:0x1\n"
      "v_mov_b32_dpp v11, v39 row_shl:14 row_mask:0xf bank_mask:0x1\n"
      "v_mov_b32_dpp v12, v40 row_shl:14 row_mask:0xf bank_mask:0x1\n"
      "v_mov_b32_dpp v13, v41 row_shl:14 row_mask:0xf bank_mask:0x1\n"
      "v_mfma_f32_16x16x32_bf16 v[52:55], v[14:17], v[236:239], v[52:55]\n"
      "s_waitcnt lgkmcnt(6)\n"
      "v_mfma_f32_16x16x32_bf16 v[48:51], v[10:13], v[26:29], v[48:51]\n"
      "v_mov_b32_dpp v18, v38 quad_perm:[0,1,2,3] row_mask:0xf bank_mask:0x4\n"
      "v_mov_b32_dpp v19, v39 quad_perm:[0,1,2,3] row_mask:0xf bank_mask:0x4\n"
      "v_mov_b32_dpp v20, v40 quad_perm:[0,1,2,3] row_mask:0xf bank_mask:0x4\n"
      "v_mov_b32_dpp v21, v41 quad_perm:[0,1,2,3] row_mask:0xf bank_mask:0x4\n"
      "s_waitcnt lgkmcnt(4)\n"
      "v_mfma_f32_16x16x32_bf16 v[52:55], v[10:13], v[30:33], v[52:55]\n"
      "v_xor_b32 v7, 0x80, v6\n"
      "ds_read_b64_tr_b16 v[26:27], v7 offset:0\n"
      "ds_read_b64_tr_b16 v[28:29], v7 offset:4096\n"
      "v_xor_b32 v8, 0xa0, v6\n"
      "ds_read_b64_tr_b16 v[30:31], v8 offset:0\n"
      "ds_read_b64_tr_b16 v[32:33], v8 offset:4096\n"
      "v_mfma_f32_16x16x32_bf16 v[48:51], v[18:21], v[240:243], v[48:51]\n"
      "v_mov_b32_dpp v22, v38 row_shr:4 row_mask:0xf bank_mask:0x8\n"
      "v_mov_b32_dpp v23, v39 row_shr:4 row_mask:0xf bank_mask:0x8\n"
      "v_mov_b32_dpp v24, v40 row_shr:4 row_mask:0xf bank_mask:0x8\n"
      "v_mov_b32_dpp v25, v41 row_shr:4 row_mask:0xf bank_mask:0x8\n"
      "v_mfma_f32_16x16x32_bf16 v[52:55], v[18:21], v[244:247], v[52:55]\n"
      "s_nop 0\n"
      "v_mfma_f32_16x16x32_bf16 v[48:51], v[22:25], v[248:251], v[48:51]\n"
      "v_mov_b32_dpp v14, v38 row_shl:10 row_mask:0xf bank_mask:0x2\n"
      "v_mov_b32_dpp v15, v39 row_shl:10 row_mask:0xf bank_mask:0x2\n"
      "v_mov_b32_dpp v16, v40 row_shl:10 row_mask:0xf bank_mask:0x2\n"
      "v_mov_b32_dpp v17, v41 row_shl:10 row_mask:0xf bank_mask:0x2\n"
      "v_mfma_f32_16x16x32_bf16 v[52:55], v[22:25], v[252:255], v[52:55]\n"
      "s_waitcnt lgkmcnt(6)\n"
      "v_mfma_f32_16x16x32_bf16 v[48:51], v[14:17], v[34:37], v[48:51]\n"
      "v_mov_b32_dpp v18, v38 row_shl:6 row_mask:0xf bank_mask:0x4\n"
      "v_mov_b32_dpp v19, v39 row_shl:6 row_mask:0xf bank_mask:0x4\n"
      "v_mov_b32_dpp v20, v40 row_shl:6 row_mask:0xf bank_mask:0x4\n"
      "v_mov_b32_dpp v21, v41 row_shl:6 row_mask:0xf bank_mask:0x4\n"
      "s_waitcnt lgkmcnt(4)\n"
      "v_mfma_f32_16x16x32_bf16 v[52:55], v[14:17], v[42:45], v[52:55]\n"
      "v_xor_b32 v7, 0xc0, v6\n"
      "ds_read_b64_tr_b16 v[34:35], v7 offset:0\n"
      "ds_read_b64_tr_b16 v[36:37], v7 offset:4096\n"
      "v_xor_b32 v8, 0xe0, v6\n"
      "ds_read_b64_tr_b16 v[42:43], v8 offset:0\n"
      "ds_read_b64_tr_b16 v[44:45], v8 offset:4096\n"
      "s_waitcnt lgkmcnt(6)\n"
      "v_mfma_f32_16x16x32_bf16 v[48:51], v[18:21], v[26:29], v[48:51]\n"
      "v_mov_b32_dpp v22, v38 row_shl:2 row_mask:0xf bank_mask:0x8\n"
      "v_mov_b32_dpp v23, v39 row_shl:2 row_mask:0xf bank_mask:0x8\n"
      "v_mov_b32_dpp v24, v40 row_shl:2 row_mask:0xf bank_mask:0x8\n"
      "v_mov_b32_dpp v25, v41 row_shl:2 row_mask:0xf bank_mask:0x8\n"
      "s_waitcnt lgkmcnt(4)\n"
      "v_mfma_f32_16x16x32_bf16 v[52:55], v[18:21], v[30:33], v[52:55]\n"
      "s_waitcnt lgkmcnt(2)\n"
      "v_mfma_f32_16x16x32_bf16 v[48:51], v[22:25], v[34:37], v[48:51]\n"
      "s_waitcnt lgkmcnt(0)\n"
      "v_mfma_f32_16x16x32_bf16 v[52:55], v[22:25], v[42:45], v[52:55]\n"
      "s_waitcnt vmcnt(0) lgkmcnt(0)\n"
      "s_nop 5\n"
      "s_branch .Lk_loop\n"
      ".Lk_end:\n"
      "s_or_b32 s60, s12, s13\n"
      "s_cmp_lg_u32 s60, 0\n"
      "s_cbranch_scc1 .Lk_fin\n"
      "s_mov_b64 exec, 0x1\n"
      "v_mov_b32 v2, s14\n"
      "v_mov_b32 v3, 0\n"
      "buffer_store_dword v2, v3, s[24:27], 0 offen sc1\n"
      "s_mov_b64 exec, -1\n"
      ".Lk_fin:\n"
      ".Lk_ze:\n"
      : "+{v0}"(tid)
      : "s"(__builtin_amdgcn_kernarg_segment_ptr()), "s"(blockIdx.x)
      : "v1", "v2", "v3", "v4", "v5", "v6", "v7", "v8", "v9", "v10", "v11", "v12", "v13", "v14", "v15", "v16", "v17", "v18", "v19", "v20", "v21", "v22", "v23", "v24", "v25", "v26", "v27", "v28", "v29", "v30", "v31", "v32", "v33", "v34", "v35", "v36", "v37", "v38", "v39", "v40", "v41", "v42", "v43", "v44", "v45", "v46", "v47", "v48", "v49", "v50", "v51", "v52", "v53", "v54", "v55", "v56", "v57", "v58", "v59", "v60", "v61", "v62", "v63", "v64", "v65", "v66", "v67", "v68", "v69", "v70", "v71", "v72", "v73", "v74", "v75", "v76", "v77", "v78", "v79", "v80", "v81", "v82", "v83", "v84", "v85", "v86", "v87", "v88", "v89", "v90", "v91", "v92", "v93", "v94", "v95", "v96", "v97", "v98", "v99", "v100", "v101", "v102", "v103", "v104", "v105", "v106", "v107", "v108", "v109", "v110", "v111", "v112", "v113", "v114", "v115", "v116", "v117", "v118", "v119", "v120", "v121", "v122", "v123", "v124", "v125", "v126", "v127", "v128", "v129", "v130", "v131", "v132", "v133", "v134", "v135", "v136", "v137", "v138", "v139", "v140", "v141", "v142", "v143", "v144", "v145", "v146", "v147", "v148", "v149", "v150", "v151", "v152", "v153", "v154", "v155", "v156", "v157", "v158", "v159", "v160", "v161", "v162", "v163", "v164", "v165", "v166", "v167", "v168", "v169", "v170", "v171", "v172", "v173", "v174", "v175", "v176", "v177", "v178", "v179", "v180", "v181", "v182", "v183", "v184", "v185", "v186", "v187", "v188", "v189", "v190", "v191", "v192", "v193", "v194", "v195", "v196", "v197", "v198", "v199", "v200", "v201", "v202", "v203", "v204", "v205", "v206", "v207", "v208", "v209", "v210", "v211", "v212", "v213", "v214", "v215", "v216", "v217", "v218", "v219", "v220", "v221", "v222", "v223", "v224", "v225", "v226", "v227", "v228", "v229", "v230", "v231", "v232", "v233", "v234", "v235", "v236", "v237", "v238", "v239", "v240", "v241", "v242", "v243", "v244", "v245", "v246", "v247", "v248", "v249", "v250", "v251", "v252", "v253", "v254", "v255", "a0", "a1", "a2", "a3", "a4", "a5", "a6", "a7", "a8", "a9", "a10", "a11", "a12", "a13", "a14", "a15", "a16", "a17", "a18", "a19", "a20", "a21", "a22", "a23", "a24", "a25", "a26", "a27", "a28", "a29", "a30", "a31", "a32", "a33", "a34", "a35", "a36", "a37", "a38", "a39", "a40", "a41", "a42", "a43", "a44", "a45", "a46", "a47", "a48", "a49", "a50", "a51", "a52", "a53", "a54", "a55", "a56", "a57", "a58", "a59", "a60", "a61", "a62", "a63", "a64", "a65", "a66", "a67", "a68", "a69", "a70", "a71", "a72", "a73", "a74", "a75", "a76", "a77", "a78", "a79", "a80", "a81", "a82", "a83", "a84", "a85", "a86", "a87", "a88", "a89", "a90", "a91", "a92", "a93", "a94", "a95", "a96", "a97", "a98", "a99", "a100", "a101", "a102", "a103", "a104", "a105", "a106", "a107", "a108", "a109", "a110", "a111", "a112", "a113", "a114", "a115", "a116", "a117", "a118", "a119", "a120", "a121", "a122", "a123", "a124", "a125", "a126", "a127", "a128", "a129", "a130", "a131", "a132", "a133", "a134", "a135", "a136", "a137", "a138", "a139", "a140", "a141", "a142", "a143", "a144", "a145", "a146", "a147", "a148", "a149", "a150", "a151", "a152", "a153", "a154", "a155", "a156", "a157", "a158", "a159", "a160", "a161", "a162", "a163", "a164", "a165", "a166", "a167", "a168", "a169", "a170", "a171", "a172", "a173", "a174", "a175", "a176", "a177", "a178", "a179", "a180", "a181", "a182", "a183", "a184", "a185", "a186", "a187", "a188", "a189", "a190", "a191", "a192", "a193", "a194", "a195", "a196", "a197", "a198", "a199", "a200", "a201", "a202", "a203", "a204", "a205", "a206", "a207", "a208", "a209", "a210", "a211", "a212", "a213", "a214", "a215", "a216", "a217", "a218", "a219", "a220", "a221", "a222", "a223", "a224", "a225", "a226", "a227", "a228", "a229", "a230", "a231", "a232", "a233", "a234", "a235", "a236", "a237", "a238", "a239", "a240", "a241", "a242", "a243", "a244", "a245", "a246", "a247", "a248", "a249", "a250", "a251", "a252", "a253", "a254", "a255", "s4", "s5", "s6", "s7", "s8", "s9", "s10", "s11", "s12", "s13", "s14", "s15", "s16", "s17", "s18", "s19", "s20", "s21", "s22", "s23", "s24", "s25", "s26", "s27", "s28", "s29", "s30", "s31", "s32", "s33", "s34", "s35", "s36", "s37", "s38", "s39", "s40", "s41", "s42", "s43", "s44", "s45", "s46", "s47", "s48", "s49", "s50", "s51", "s52", "s53", "s54", "s55", "s56", "s57", "s58", "s59", "s60", "s61", "s62", "s63", "s64", "s65", "s66", "s67", "s68", "s69", "s70", "s71", "s72", "s73", "s74", "s75", "s76", "s77", "s78", "s79", "s80", "s81", "s82", "s83", "s84", "s85", "s86", "s87", "s88", "s89", "s90", "s91", "s92", "s93", "s94", "s95", "s96", "s97", "s98", "s99", "vcc", "scc", "memory");
}

extern "C" void kernel_launch(void* const* d_in, const int* in_sizes, int n_in, void* d_out, int out_size, void* d_ws,
                              size_t ws_size, hipStream_t stream) {
  (void)in_sizes; (void)n_in; (void)out_size; (void)ws_size;
  (void)hipFuncSetAttribute((const void*)fused_decode, hipFuncAttributeMaxDynamicSharedMemorySize, 163840);
  fused_decode<<<256, 256, 163840, stream>>>(d_in[0], d_in[1], d_in[2], d_in[3], d_in[4], d_in[6], d_in[7], d_in[8], d_out, d_ws);
}
```
